```python
import torch

def _softmax(x, axis=-1):
    return torch.softmax(x, dim=axis)
def _gelu(x):
    return torch.nn.functional.gelu(x, approximate="tanh")
def _silu(x):
    return torch.nn.functional.silu(x)
def _sigmoid(x):
    return torch.sigmoid(x)
def _layernorm(x, axis=-1, eps=1e-5):
    m = x.mean(dim=axis, keepdim=True)
    v = x.var(dim=axis, keepdim=True, unbiased=False)
    return (x - m) / torch.sqrt(v + eps)
def _scatter_add(tgt, idx, src, axis=0):
    return tgt.clone().index_add_(axis, idx, src)
def _segment_sum(src, seg, n):
    out = torch.zeros((n,) + src.shape[1:], dtype=src.dtype)
    return out.index_add_(0, seg, src)
def _pad(x, pw):
    flat = tuple(v for pair in reversed(pw) for v in pair)
    return torch.nn.functional.pad(x, flat)

kernel_name = 'standalone__gqa_softmax__near_ridge'

def setup_inputs():

    _w = (lambda a: (lambda t: t.to(torch.bfloat16) if t.is_floating_point() else t)(torch.as_tensor(a)))
    B, Hq, Hkv, Sq, Sk, D = 32, 32, 4, 256, 256, 128
    return {
        "q": _w((lambda *s: torch.randn(*s, dtype=torch.bfloat16) * 0.8)(B, Hq, Sq, D)),
        "k": _w((lambda *s: torch.randn(*s, dtype=torch.bfloat16) * 0.8)(B, Hkv, Sk, D)),
        "v": _w((lambda *s: torch.randn(*s, dtype=torch.bfloat16) * 0.05)(B, Hkv, Sk, D)),
    }

def reference(q, k, v):
    B, Hq, Sq, D = q.shape
    Hkv = k.shape[1]
    rep = Hq // Hkv
    k = (lambda x, n, axis: torch.repeat_interleave(x, n, dim=axis))(k, rep, axis=1)
    v = (lambda x, n, axis: torch.repeat_interleave(x, n, dim=axis))(v, rep, axis=1)
    scale = D ** -0.5
    scores = torch.einsum("bhqd,bhkd->bhqk", q, k) * scale
    p = _softmax(scores, axis=-1)
    return torch.einsum("bhqk,bhkd->bhqd", p, v)
```

```cpp
#include <hip/hip_runtime.h>
#include <hip/hip_bf16.h>

typedef float    f32x16 __attribute__((ext_vector_type(16)));
typedef float    f32x4  __attribute__((ext_vector_type(4)));
typedef float    f32x2  __attribute__((ext_vector_type(2)));
typedef __bf16   bf16x8 __attribute__((ext_vector_type(8)));
typedef __bf16   bf16x4 __attribute__((ext_vector_type(4)));
typedef __bf16   bf16x2 __attribute__((ext_vector_type(2)));
typedef unsigned u32x4  __attribute__((ext_vector_type(4)));
typedef unsigned u32x2  __attribute__((ext_vector_type(2)));
#define DI __device__ __forceinline__
#define LDS3 __attribute__((address_space(3)))

constexpr int KOFF = 0, VOFF = 65536, SOFF = 131072;
constexpr int LDS_BYTES = SOFF + 8 * 128 + 64;
constexpr float CSCALE = 0.12751743082f;
constexpr float TBOUND = 40.f;

DI unsigned cvtpk(float lo, float hi) { f32x2 v = {lo, hi}; bf16x2 b = __builtin_convertvector(v, bf16x2); return __builtin_bit_cast(unsigned, b); }
DI float bflo(unsigned w) { return __uint_as_float(w << 16); }
DI float bfhi(unsigned w) { return __uint_as_float(w & 0xffff0000u); }
DI float swapadd(float x) { auto r = __builtin_amdgcn_permlane32_swap(__float_as_uint(x), __float_as_uint(x), false, false); return __uint_as_float(r[0]) + __uint_as_float(r[1]); }
DI float swapmax(float x) { auto r = __builtin_amdgcn_permlane32_swap(__float_as_uint(x), __float_as_uint(x), false, false); return fmaxf(__uint_as_float(r[0]), __uint_as_float(r[1])); }
DI float fmul_s(float a, float b) { float r; asm("v_mul_f32_e32 %0, %1, %2" : "=v"(r) : "v"(a), "v"(b)); return r; }
DI float fadd_s(float a, float b) { float r; asm("v_add_f32_e32 %0, %1, %2" : "=v"(r) : "v"(a), "v"(b)); return r; }
#define MFMA(a, b, c) __builtin_amdgcn_mfma_f32_32x32x16_bf16((a), (b), (c), 0, 0, 0)

DI f32x16 qk_tile(const char* lds, unsigned kb0, unsigned kb1, int kb, const bf16x8 (&q)[8]) {
  f32x16 s = {};
  #pragma unroll
  for (int t = 0; t < 8; ++t) {
    const bf16x8 a = *(const bf16x8*)(lds + ((t & 1) ? kb1 : kb0) + kb * 8192 + (t >> 1) * 512);
    s = MFMA(a, q[t], s);
  }
  return s;
}
DI bf16x8 vfrag(const char* lds, unsigned vb, int kb, int s, int i) {
  typedef LDS3 bf16x4* lp;
  const bf16x4 lo = __builtin_amdgcn_ds_read_tr16_b64_v4bf16((lp)(lds + vb + (kb * 8 + 4 * s) * 1024 + i * 256));
  const bf16x4 hi = __builtin_amdgcn_ds_read_tr16_b64_v4bf16((lp)(lds + vb + (kb * 8 + 4 * s + 2) * 1024 + i * 256));
  return __builtin_shufflevector(lo, hi, 0, 1, 2, 3, 4, 5, 6, 7);
}
#define SB() __builtin_amdgcn_sched_barrier(0)
template <bool SUB>
DI void sm_pv(const char* lds, unsigned vb, f32x16& s, float m, f32x16 (&o)[4], float& l0, float& l1) {
    #pragma unroll
    for (int r = 0; r < 16; ++r) s[r] = __builtin_amdgcn_exp2f(SUB ? s[r] - m : s[r]);
    #pragma unroll
    for (int r = 0; r < 16; r += 2) { l0 = fadd_s(l0, s[r]); l1 = fadd_s(l1, s[r + 1]); }
    #pragma unroll
    for (int h = 0; h < 2; ++h) {
      u32x4 w;
      #pragma unroll
      for (int e = 0; e < 4; ++e) w[e] = cvtpk(s[8 * h + 2 * e], s[8 * h + 2 * e + 1]);
      const bf16x8 p = __builtin_bit_cast(bf16x8, w);
      #pragma unroll
      for (int i = 0; i < 4; ++i) o[i] = MFMA(p, vfrag(lds, vb, 0, h, i), o[i]);
    }
}
template <bool SUB>
DI void block_main(const char* lds, unsigned kb0, unsigned kb1, unsigned vb, const bf16x8 (&q)[8], float m, f32x16 (&o)[4], float& lsum) {
  f32x16 sA = qk_tile(lds, kb0, kb1, 0, q), sB;
  float l0 = 0.f, l1 = 0.f;
  #pragma unroll 1
  for (int kb = 0; kb < 8; kb += 2) {
    SB();
    sB = qk_tile(lds, kb0, kb1, 1, q);
    SB();
    sm_pv<SUB>(lds, vb, sA, m, o, l0, l1);
    SB();
    if (kb < 6) sA = qk_tile(lds, kb0, kb1, 2, q);
    SB();
    sm_pv<SUB>(lds, vb + 8192, sB, m, o, l0, l1);
    kb0 += 16384; kb1 += 16384; vb += 16384;
  }
  lsum = l0 + l1;
}

__global__ __launch_bounds__(512, 2) void gqa_attn(const unsigned short* __restrict__ Q, const unsigned short* __restrict__ K,
                                                   const unsigned short* __restrict__ V, unsigned short* __restrict__ O) {
  __shared__ __attribute__((aligned(16))) char lds[LDS_BYTES];
  const int bid = blockIdx.x, tid = threadIdx.x, lane = tid & 63;
  const int wid = __builtin_amdgcn_readfirstlane(tid >> 6);
  const int g = (bid & 7) + 8 * (bid >> 4), half = (bid >> 3) & 1;
  const int r32 = lane & 31, h = lane >> 5;
  const unsigned short* Qg = Q + ((size_t)g * 2048 + half * 1024) * 128;
  unsigned short* Og = O + ((size_t)g * 2048 + half * 1024) * 128;
  bf16x8 q[8];
  const u32x4* Kg = (const u32x4*)(K + (size_t)g * 256 * 128); const u32x4* Vg = (const u32x4*)(V + (size_t)g * 256 * 128);
  u32x4 kr[8], vr[8];
  #pragma unroll
  for (int i = 0; i < 8; ++i) kr[i] = Kg[i * 512 + tid];
  {
    const unsigned short* qp = Qg + (size_t)(wid * 32 + r32) * 128 + h * 8;
    #pragma unroll
    for (int t = 0; t < 8; ++t) q[t] = *(const bf16x8*)(qp + t * 16);
  }
  #pragma unroll
  for (int i = 0; i < 8; ++i) vr[i] = Vg[i * 512 + tid];
  float kmax2 = 0.f, vmax = 0.f;
  {
    const int ch = tid & 15;
    #pragma unroll
    for (int i = 0; i < 8; ++i) {
      const int row = i * 32 + (tid >> 4);
      u32x4 w; float n2 = 0.f;
      #pragma unroll
      for (int e = 0; e < 4; ++e) {
        w[e] = cvtpk(bflo(kr[i][e]) * CSCALE, bfhi(kr[i][e]) * CSCALE);
        const float a = bflo(w[e]), b = bfhi(w[e]); n2 += a * a + b * b;
      }
      *(u32x4*)(lds + KOFF + 2048 * (row >> 3) + 512 * (ch >> 2) + 64 * (row & 7) + 16 * ((ch & 3) ^ ((row >> 2) & 3))) = w;
      n2 += __shfl_xor(n2, 1); n2 += __shfl_xor(n2, 2); n2 += __shfl_xor(n2, 4); n2 += __shfl_xor(n2, 8);
      kmax2 = !(n2 <= kmax2) ? n2 : kmax2;
    }
    #pragma unroll
    for (int i = 0; i < 8; ++i) {
      const int key = i * 32 + (tid >> 4);
      char* p = lds + VOFF + (key >> 2) * 1024 + (key & 3) * 64 + 4 * ch;
      *(unsigned*)(p + 0)   = __builtin_amdgcn_perm(vr[i][2], vr[i][0], 0x05040100);
      *(unsigned*)(p + 256) = __builtin_amdgcn_perm(vr[i][2], vr[i][0], 0x07060302);
      *(unsigned*)(p + 512) = __builtin_amdgcn_perm(vr[i][3], vr[i][1], 0x05040100);
      *(unsigned*)(p + 768) = __builtin_amdgcn_perm(vr[i][3], vr[i][1], 0x07060302);
      #pragma unroll
      for (int e = 0; e < 4; ++e) { vmax = fmaxf(vmax, fmaxf(fabsf(bflo(vr[i][e])), fabsf(bfhi(vr[i][e])))); }
    }
    #pragma unroll
    for (int o = 1; o < 64; o <<= 1) { const float a = __shfl_xor(kmax2, o); kmax2 = !(a <= kmax2) ? a : kmax2; vmax = fmaxf(vmax, __shfl_xor(vmax, o)); }
    float* red = (float*)(lds + SOFF + 1024);
    if (lane == 0) { red[wid] = kmax2; red[8 + wid] = vmax; }
  }
  __syncthreads();
  {
    const float* red = (const float*)(lds + SOFF + 1024);
    kmax2 = red[0]; vmax = red[8];
    #pragma unroll
    for (int i = 1; i < 8; ++i) { const float a = red[i]; kmax2 = !(a <= kmax2) ? a : kmax2; vmax = fmaxf(vmax, red[8 + i]); }
    if (!(vmax <= 0x1p60f) || vmax < 0x1p-40f) kmax2 = __builtin_inff();
  }
  const unsigned kb0 = KOFF + 2048 * (r32 >> 3) + 64 * (r32 & 7) + 16 * ((0 + h) ^ ((r32 >> 2) & 3));
  const unsigned kb1 = KOFF + 2048 * (r32 >> 3) + 64 * (r32 & 7) + 16 * ((2 + h) ^ ((r32 >> 2) & 3));
  const unsigned vb = VOFF + h * 1024 + ((lane >> 2) & 3) * 64 + ((lane >> 4) & 1) * 32 + (lane & 3) * 8;
  float* lsc = (float*)(lds + SOFF + wid * 128);

  #pragma unroll 1
  for (int it = 0; it < 4; ++it) {
    const int row0 = (it * 8 + wid) * 32;
    bf16x8 qn[8];
    if (it < 3) {
      const unsigned short* qp = Qg + (size_t)(row0 + 256 + r32) * 128 + h * 8;
      #pragma unroll
      for (int t = 0; t < 8; ++t) qn[t] = *(const bf16x8*)(qp + t * 16);
    }
    float n2 = 0.f;
    #pragma unroll
    for (int t = 0; t < 8; ++t) {
      const u32x4 w = __builtin_bit_cast(u32x4, q[t]);
      #pragma unroll
      for (int e = 0; e < 4; ++e) { const bf16x2 x = __builtin_bit_cast(bf16x2, w[e]); n2 = __builtin_amdgcn_fdot2_f32_bf16(x, x, n2, false); }
    }
    n2 = swapadd(n2);
    const bool safe = __all(n2 * kmax2 <= TBOUND * TBOUND * 0.98f);
    f32x16 o[4] = {};
    float l;
    if (__builtin_expect(safe, 1)) {
      block_main<false>(lds, kb0, kb1, vb, q, 0.f, o, l);
    } else {
      float m = -__builtin_inff();
      #pragma unroll 1
      for (int kb = 0; kb < 8; ++kb) {
        const f32x16 s = qk_tile(lds, kb0, kb1, kb, q);
        #pragma unroll
        for (int r = 0; r < 16; ++r) m = fmaxf(m, s[r]);
      }
      m = swapmax(m);
      block_main<true>(lds, kb0, kb1, vb, q, m, o, l);
    }
    l = swapadd(l);
    lsc[r32] = __builtin_amdgcn_rcpf(l);
    f32x4 rl[4];
    #pragma unroll
    for (int gq = 0; gq < 4; ++gq) rl[gq] = *(const f32x4*)(lsc + 8 * gq + 4 * h);
    char* op = (char*)Og + (size_t)(row0 + 4 * h) * 256 + r32 * 8;
    #pragma unroll
    for (int r = 0; r < 16; ++r) {
      const float f = rl[r >> 2][r & 3];
      u32x2 w = {cvtpk(fmul_s(o[0][r], f), fmul_s(o[1][r], f)), cvtpk(fmul_s(o[2][r], f), fmul_s(o[3][r], f))};
      *(u32x2*)(op + ((r & 3) + 8 * (r >> 2)) * 256) = w;
    }
    if (it < 3) {
      #pragma unroll
      for (int t = 0; t < 8; ++t) q[t] = qn[t];
    }
  }
}

extern "C" void kernel_launch(void* const* d_in, const int* in_sizes, int n_in, void* d_out, int out_size, void* d_ws, size_t ws_size, hipStream_t stream) {
  gqa_attn<<<256, 512, 0, stream>>>((const unsigned short*)d_in[0], (const unsigned short*)d_in[1], (const unsigned short*)d_in[2], (unsigned short*)d_out);
}
```

```cpp
#include <hip/hip_runtime.h>
#include <hip/hip_bf16.h>

__global__ __launch_bounds__(256, 1) void standalone__gqa_softmax__near_ridge_kernel(const void* __restrict__ q, const void* __restrict__ k, const void* __restrict__ v, void* __restrict__ o) {
  __shared__ __attribute__((aligned(16))) char lds[163840];
  unsigned tid = threadIdx.x;
  unsigned ldsaddr = (unsigned)(size_t)(__attribute__((address_space(3))) char*)lds;
  const void* ka = (const void*)__builtin_amdgcn_kernarg_segment_ptr();
  asm volatile(
    "s_load_dwordx8 s[8:15], s[2:3], 0x0\n"
    "v_lshrrev_b32 v1, 6, v0\n"
    "v_and_b32 v0, 63, v0\n"
    "v_readfirstlane_b32 s36, v1\n"
    "s_and_b32 s17, s16, 7\n"
    "s_lshr_b32 s18, s16, 4\n"
    "s_lshl_b32 s18, s18, 3\n"
    "s_add_u32 s17, s17, s18\n"
    "s_lshl_b32 s19, s17, 16\n"
    "v_lshrrev_b32 v2, 5, v0\n"
    "v_bfe_u32 v6, v0, 2, 3\n"
    "v_lshlrev_b32 v6, 8, v6\n"
    "v_lshl_add_u32 v6, v2, 6, v6\n"
    "v_bfe_u32 v7, v0, 4, 1\n"
    "v_and_b32 v8, 3, v0\n"
    "v_xor_b32 v9, v8, v7\n"
    "v_lshl_add_u32 v248, v9, 4, v6\n"
    "v_xor_b32 v9, 2, v9\n"
    "v_lshl_add_u32 v249, v9, 4, v6\n"
    "s_and_b32 s58, s36, 1\n"
    "s_lshl_b32 s58, s58, 1\n"
    "v_xor_b32 v9, s58, v8\n"
    "v_xor_b32 v9, v9, v7\n"
    "v_lshl_add_u32 v232, v9, 4, v6\n"
    "s_lshl_b32 s56, s36, 11\n"
    "s_mov_b32 s26, 0x10000\n"
    "s_mov_b32 s27, 0x20000\n"
    "s_waitcnt lgkmcnt(0)\n"
    "s_add_u32 s24, s10, s19\n"
    "s_addc_u32 s25, s11, 0\n"
    "s_add_u32 s58, s56, 0\n"
    "s_mov_b32 m0, s58\n"
    "s_nop 0\n"
    "buffer_load_dwordx4 v232, s[24:27], s58 offen offset:0 nt lds\n"
    "s_add_u32 m0, s58, 896\n"
    "s_nop 0\n"
    "buffer_load_dwordx4 v232, s[24:27], s58 offen offset:128 nt lds\n"
    "s_add_u32 s58, s56, 8192\n"
    "s_mov_b32 m0, s58\n"
    "s_nop 0\n"
    "buffer_load_dwordx4 v232, s[24:27], s58 offen offset:0 nt lds\n"
    "s_add_u32 m0, s58, 896\n"
    "s_nop 0\n"
    "buffer_load_dwordx4 v232, s[24:27], s58 offen offset:128 nt lds\n"
    "s_add_u32 s28, s12, s19\n"
    "s_addc_u32 s29, s13, 0\n"
    "s_mov_b32 s30, 0x10000\n"
    "s_mov_b32 s31, 0x20000\n"
    "s_lshr_b32 s18, s16, 3\n"
    "s_and_b32 s18, s18, 1\n"
    "s_lshl_b32 s19, s17, 1\n"
    "s_add_u32 s19, s19, s18\n"
    "s_lshl_b32 s19, s19, 18\n"
    "s_add_u32 s20, s8, s19\n"
    "s_addc_u32 s21, s9, 0\n"
    "s_mov_b32 s22, 0x40000\n"
    "s_mov_b32 s23, 0x20000\n"
    "s_lshl_b32 s37, s36, 13\n"
    "s_add_u32 s40, s37, 65536\n"
    "s_add_u32 s42, s37, 4096\n"
    "s_add_u32 m0, s40, 65536\n"
    "s_nop 0\n"
    "buffer_load_dwordx4 v248, s[20:23], s37 offen offset:0 nt lds\n"
    "buffer_load_dwordx4 v249, s[20:23], s37 offen offset:2048 nt lds\n"
    "s_add_u32 m0, s40, 66432\n"
    "s_nop 0\n"
    "buffer_load_dwordx4 v248, s[20:23], s37 offen offset:128 nt lds\n"
    "buffer_load_dwordx4 v249, s[20:23], s37 offen offset:2176 nt lds\n"
    "s_add_u32 m0, s40, 69632\n"
    "s_nop 0\n"
    "buffer_load_dwordx4 v248, s[20:23], s42 offen offset:0 nt lds\n"
    "buffer_load_dwordx4 v249, s[20:23], s42 offen offset:2048 nt lds\n"
    "s_add_u32 m0, s40, 70528\n"
    "s_nop 0\n"
    "buffer_load_dwordx4 v248, s[20:23], s42 offen offset:128 nt lds\n"
    "buffer_load_dwordx4 v249, s[20:23], s42 offen offset:2176 nt lds\n"
    "s_getpc_b64 s[58:59]\n"
    "Lpc_%=:\n"
    "s_mul_i32 s18, s36, 4608\n"
    "s_add_u32 s58, s58, Lfirst_%= - Lpc_%=\n"
    "s_addc_u32 s59, s59, 0\n"
    "s_add_u32 s58, s58, s18\n"
    "s_addc_u32 s59, s59, 0\n"
    "v_lshlrev_b32 v10, 6, v0\n"
    "global_load_dword v16, v10, s[58:59]\n"
    "s_add_u32 s58, s58, 4096\n"
    "s_addc_u32 s59, s59, 0\n"
    "global_load_dword v17, v10, s[58:59]\n"
    "v_and_b32 v3, 31, v0\n"
    "v_lshlrev_b32 v4, 10, v2\n"
    "v_lshl_add_u32 v250, v3, 3, v4\n"
    "s_add_u32 s59, s56, 0\n"
    "buffer_load_dwordx2 v[64:65], v250, s[28:31], s59 offen offset:0 nt\n"
    "buffer_load_dwordx2 v[66:67], v250, s[28:31], s59 offen offset:256 nt\n"
    "buffer_load_dwordx2 v[68:69], v250, s[28:31], s59 offen offset:512 nt\n"
    "buffer_load_dwordx2 v[70:71], v250, s[28:31], s59 offen offset:768 nt\n"
    "s_add_u32 s58, s56, 16384\n"
    "s_mov_b32 m0, s58\n"
    "s_nop 0\n"
    "buffer_load_dwordx4 v232, s[24:27], s58 offen offset:0 nt lds\n"
    "s_add_u32 m0, s58, 896\n"
    "s_nop 0\n"
    "buffer_load_dwordx4 v232, s[24:27], s58 offen offset:128 nt lds\n"
    "s_add_u32 s48, s14, s19\n"
    "s_addc_u32 s49, s15, 0\n"
    "s_mov_b32 s50, 0x40000\n"
    "s_mov_b32 s51, 0x20000\n"
    "v_lshrrev_b32 v4, 3, v3\n"
    "v_lshlrev_b32 v4, 11, v4\n"
    "v_and_b32 v5, 7, v3\n"
    "v_lshl_add_u32 v4, v5, 6, v4\n"
    "v_bfe_u32 v5, v3, 2, 2\n"
    "v_xor_b32 v6, v2, v5\n"
    "v_lshl_add_u32 v244, v6, 4, v4\n"
    "v_xor_b32 v6, 2, v6\n"
    "v_lshl_add_u32 v245, v6, 4, v4\n"
    "v_add_u32 v246, s40, v244\n"
    "v_add_u32 v247, s40, v245\n"
    "v_add_u32 v12, 0x10000, v246\n"
    "v_add_u32 v13, 0x10000, v247\n"
    "s_lshl_b32 s58, s36, 7\n"
    "s_add_u32 s58, s58, 133120\n"
    "v_lshlrev_b32 v4, 2, v3\n"
    "v_add_u32 v251, s58, v4\n"
    "v_lshlrev_b32 v4, 4, v2\n"
    "v_add_u32 v252, s58, v4\n"
    "s_mov_b32 s53, 0x05040100\n"
    "s_mov_b32 s54, 0x07060302\n"
    "s_mov_b32 s55, 0x3e0293ee\n"
    "v_lshlrev_b32 v11, 4, v0\n"
    "v_add_u32 v233, s56, v11\n"
    "v_mov_b32 v234, 0\n"
    "s_lshr_b32 s58, s36, 1\n"
    "s_lshl_b32 s58, s58, 12\n"
    "s_and_b32 s59, s36, 1\n"
    "s_lshl_b32 s59, s59, 3\n"
    "s_add_u32 s58, s58, s59\n"
    "s_add_u32 s58, s58, 131072\n"
    "v_add_u32 v235, s58, v11\n"
    "v_add_u32 v236, 131072, v11\n"
    "s_add_u32 s41, s37, 98304\n"
    "s_add_u32 s42, s41, 4096\n"
    "s_sub_u32 s45, s37, 32768\n"
    "s_mov_b32 s52, 0\n"
    "s_waitcnt vmcnt(18)\n"
    "ds_read_b128 v[216:219], v233 offset:0\n"
    "ds_read_b128 v[220:223], v233 offset:1024\n"
    "s_waitcnt lgkmcnt(1)\n"
    "v_lshlrev_b32 v224, 16, v216\n"
    "v_and_b32 v225, 0xffff0000, v216\n"
    "v_mul_f32 v224, s55, v224\n"
    "v_mul_f32 v225, s55, v225\n"
    "v_cvt_pk_bf16_f32 v216, v224, v225\n"
    "v_lshlrev_b32 v224, 16, v217\n"
    "v_and_b32 v225, 0xffff0000, v217\n"
    "v_mul_f32 v224, s55, v224\n"
    "v_mul_f32 v225, s55, v225\n"
    "v_cvt_pk_bf16_f32 v217, v224, v225\n"
    "v_lshlrev_b32 v224, 16, v218\n"
    "v_and_b32 v225, 0xffff0000, v218\n"
    "v_mul_f32 v224, s55, v224\n"
    "v_mul_f32 v225, s55, v225\n"
    "v_cvt_pk_bf16_f32 v218, v224, v225\n"
    "v_lshlrev_b32 v224, 16, v219\n"
    "v_and_b32 v225, 0xffff0000, v219\n"
    "v_mul_f32 v224, s55, v224\n"
    "v_mul_f32 v225, s55, v225\n"
    "v_cvt_pk_bf16_f32 v219, v224, v225\n"
    "ds_write_b128 v233, v[216:219] offset:0\n"
    "s_waitcnt lgkmcnt(1)\n"
    "v_lshlrev_b32 v224, 16, v220\n"
    "v_and_b32 v225, 0xffff0000, v220\n"
    "v_mul_f32 v224, s55, v224\n"
    "v_mul_f32 v225, s55, v225\n"
    "v_cvt_pk_bf16_f32 v220, v224, v225\n"
    "v_lshlrev_b32 v224, 16, v221\n"
    "v_and_b32 v225, 0xffff0000, v221\n"
    "v_mul_f32 v224, s55, v224\n"
    "v_mul_f32 v225, s55, v225\n"
    "v_cvt_pk_bf16_f32 v221, v224, v225\n"
    "v_lshlrev_b32 v224, 16, v222\n"
    "v_and_b32 v225, 0xffff0000, v222\n"
    "v_mul_f32 v224, s55, v224\n"
    "v_mul_f32 v225, s55, v225\n"
    "v_cvt_pk_bf16_f32 v222, v224, v225\n"
    "v_lshlrev_b32 v224, 16, v223\n"
    "v_and_b32 v225, 0xffff0000, v223\n"
    "v_mul_f32 v224, s55, v224\n"
    "v_mul_f32 v225, s55, v225\n"
    "v_cvt_pk_bf16_f32 v223, v224, v225\n"
    "ds_write_b128 v233, v[220:223] offset:1024\n"
    "s_add_u32 s59, s56, 8192\n"
    "buffer_load_dwordx2 v[72:73], v250, s[28:31], s59 offen offset:0 nt\n"
    "buffer_load_dwordx2 v[74:75], v250, s[28:31], s59 offen offset:256 nt\n"
    "buffer_load_dwordx2 v[76:77], v250, s[28:31], s59 offen offset:512 nt\n"
    "buffer_load_dwordx2 v[78:79], v250, s[28:31], s59 offen offset:768 nt\n"
    "s_add_u32 s58, s56, 24576\n"
    "s_mov_b32 m0, s58\n"
    "s_nop 0\n"
    "buffer_load_dwordx4 v232, s[24:27], s58 offen offset:0 nt lds\n"
    "s_add_u32 m0, s58, 896\n"
    "s_nop 0\n"
    "buffer_load_dwordx4 v232, s[24:27], s58 offen offset:128 nt lds\n"
    "s_waitcnt vmcnt(22)\n"
    "ds_read_b128 v[216:219], v233 offset:8192\n"
    "ds_read_b128 v[220:223], v233 offset:9216\n"
    "s_waitcnt lgkmcnt(1)\n"
    "v_lshlrev_b32 v224, 16, v216\n"
    "v_and_b32 v225, 0xffff0000, v216\n"
    "v_mul_f32 v224, s55, v224\n"
    "v_mul_f32 v225, s55, v225\n"
    "v_cvt_pk_bf16_f32 v216, v224, v225\n"
    "v_lshlrev_b32 v224, 16, v217\n"
    "v_and_b32 v225, 0xffff0000, v217\n"
    "v_mul_f32 v224, s55, v224\n"
    "v_mul_f32 v225, s55, v225\n"
    "v_cvt_pk_bf16_f32 v217, v224, v225\n"
    "v_lshlrev_b32 v224, 16, v218\n"
    "v_and_b32 v225, 0xffff0000, v218\n"
    "v_mul_f32 v224, s55, v224\n"
    "v_mul_f32 v225, s55, v225\n"
    "v_cvt_pk_bf16_f32 v218, v224, v225\n"
    "v_lshlrev_b32 v224, 16, v219\n"
    "v_and_b32 v225, 0xffff0000, v219\n"
    "v_mul_f32 v224, s55, v224\n"
    "v_mul_f32 v225, s55, v225\n"
    "v_cvt_pk_bf16_f32 v219, v224, v225\n"
    "ds_write_b128 v233, v[216:219] offset:8192\n"
    "s_waitcnt lgkmcnt(1)\n"
    "v_lshlrev_b32 v224, 16, v220\n"
    "v_and_b32 v225, 0xffff0000, v220\n"
    "v_mul_f32 v224, s55, v224\n"
    "v_mul_f32 v225, s55, v225\n"
    "v_cvt_pk_bf16_f32 v220, v224, v225\n"
    "v_lshlrev_b32 v224, 16, v221\n"
    "v_and_b32 v225, 0xffff0000, v221\n"
    "v_mul_f32 v224, s55, v224\n"
    "v_mul_f32 v225, s55, v225\n"
    "v_cvt_pk_bf16_f32 v221, v224, v225\n"
    "v_lshlrev_b32 v224, 16, v222\n"
    "v_and_b32 v225, 0xffff0000, v222\n"
    "v_mul_f32 v224, s55, v224\n"
    "v_mul_f32 v225, s55, v225\n"
    "v_cvt_pk_bf16_f32 v222, v224, v225\n"
    "v_lshlrev_b32 v224, 16, v223\n"
    "v_and_b32 v225, 0xffff0000, v223\n"
    "v_mul_f32 v224, s55, v224\n"
    "v_mul_f32 v225, s55, v225\n"
    "v_cvt_pk_bf16_f32 v223, v224, v225\n"
    "ds_write_b128 v233, v[220:223] offset:9216\n"
    "s_add_u32 s59, s56, 16384\n"
    "buffer_load_dwordx2 v[80:81], v250, s[28:31], s59 offen offset:0 nt\n"
    "buffer_load_dwordx2 v[82:83], v250, s[28:31], s59 offen offset:256 nt\n"
    "buffer_load_dwordx2 v[84:85], v250, s[28:31], s59 offen offset:512 nt\n"
    "buffer_load_dwordx2 v[86:87], v250, s[28:31], s59 offen offset:768 nt\n"
    "s_add_u32 s58, s56, 32768\n"
    "s_mov_b32 m0, s58\n"
    "s_nop 0\n"
    "buffer_load_dwordx4 v232, s[24:27], s58 offen offset:0 nt lds\n"
    "s_add_u32 m0, s58, 896\n"
    "s_nop 0\n"
    "buffer_load_dwordx4 v232, s[24:27], s58 offen offset:128 nt lds\n"
    "s_add_u32 s59, s56, 24576\n"
    "buffer_load_dwordx2 v[88:89], v250, s[28:31], s59 offen offset:0 nt\n"
    "buffer_load_dwordx2 v[90:91], v250, s[28:31], s59 offen offset:256 nt\n"
    "buffer_load_dwordx2 v[92:93], v250, s[28:31], s59 offen offset:512 nt\n"
    "buffer_load_dwordx2 v[94:95], v250, s[28:31], s59 offen offset:768 nt\n"
    "s_waitcnt vmcnt(24)\n"
    "ds_read_b128 v[128:131], v12 offset:0\n"
    "ds_read_b128 v[132:135], v13 offset:0\n"
    "ds_read_b128 v[136:139], v12 offset:512\n"
    "ds_read_b128 v[140:143], v13 offset:512\n"
    "ds_read_b128 v[144:147], v12 offset:1024\n"
    "ds_read_b128 v[148:151], v13 offset:1024\n"
    "ds_read_b128 v[152:155], v12 offset:1536\n"
    "ds_read_b128 v[156:159], v13 offset:1536\n"
    "s_waitcnt lgkmcnt(0)\n"
    "s_barrier\n"
    "Lfirst_%=:\n"
    "ds_read_b128 v[192:195], v244 offset:0\n"
    "s_add_u32 s58, s56, 40960\n"
    "s_mov_b32 m0, s58\n"
    "s_nop 0\n"
    "buffer_load_dwordx4 v232, s[24:27], s58 offen offset:0 nt lds\n"
    "s_add_u32 m0, s58, 896\n"
    "s_nop 0\n"
    "buffer_load_dwordx4 v232, s[24:27], s58 offen offset:128 nt lds\n"
    "ds_read_b128 v[196:199], v245 offset:0\n"
    "s_add_u32 s59, s56, 32768\n"
    "buffer_load_dwordx2 v[96:97], v250, s[28:31], s59 offen offset:0 nt\n"
    "buffer_load_dwordx2 v[98:99], v250, s[28:31], s59 offen offset:256 nt\n"
    "buffer_load_dwordx2 v[100:101], v250, s[28:31], s59 offen offset:512 nt\n"
    "ds_read_b128 v[200:203], v244 offset:512\n"
    "s_waitcnt vmcnt(23)\n"
    "v_perm_b32 v226, v66, v64, s53\n"
    "v_perm_b32 v227, v70, v68, s53\n"
    "v_max3_f32 v234, |v226|, |v227|, v234\n"
    "ds_write_b64 v235, v[226:227] offset:0\n"
    "v_perm_b32 v228, v66, v64, s54\n"
    "v_perm_b32 v229, v70, v68, s54\n"
    "ds_write_b64 v235, v[228:229] offset:1024\n"
    "v_perm_b32 v226, v67, v65, s53\n"
    "ds_read_b128 v[204:207], v245 offset:512\n"
    "v_perm_b32 v227, v71, v69, s53\n"
    "v_max3_f32 v234, |v226|, |v227|, v234\n"
    "ds_write_b64 v235, v[226:227] offset:2048\n"
    "v_perm_b32 v228, v67, v65, s54\n"
    "v_perm_b32 v229, v71, v69, s54\n"
    "ds_write_b64 v235, v[228:229] offset:3072\n"
    "v_max3_f32 v234, |v64|, |v65|, v234\n"
    "v_max3_f32 v234, |v66|, |v67|, v234\n"
    "s_waitcnt lgkmcnt(6)\n"
    "v_mfma_f32_32x32x16_bf16 v[160:175], v[192:195], v[128:131], 0\n"
    "ds_read_b128 v[192:195], v244 offset:1024\n"
    "v_max3_f32 v234, |v68|, |v69|, v234\n"
    "v_max3_f32 v234, |v70|, |v71|, v234\n"
    "v_lshlrev_b32 v230, 16, v64\n"
    "v_lshlrev_b32 v231, 16, v65\n"
    "v_max3_f32 v234, |v230|, |v231|, v234\n"
    "v_lshlrev_b32 v230, 16, v68\n"
    "v_lshlrev_b32 v231, 16, v69\n"
    "v_max3_f32 v234, |v230|, |v231|, v234\n"
    "v_mfma_f32_32x32x16_bf16 v[160:175], v[196:199], v[132:135], v[160:175]\n"
    "ds_read_b128 v[196:199], v245 offset:1024\n"
    "buffer_load_dwordx2 v[102:103], v250, s[28:31], s59 offen offset:768 nt\n"
    "s_waitcnt lgkmcnt(4)\n"
    "v_mfma_f32_32x32x16_bf16 v[160:175], v[200:203], v[136:139], v[160:175]\n"
    "ds_read_b128 v[200:203], v244 offset:1536\n"
    "v_mfma_f32_32x32x16_bf16 v[160:175], v[204:207], v[140:143], v[160:175]\n"
    "ds_read_b128 v[204:207], v245 offset:1536\n"
    "s_waitcnt lgkmcnt(2)\n"
    "v_mfma_f32_32x32x16_bf16 v[160:175], v[192:195], v[144:147], v[160:175]\n"
    "v_mfma_f32_32x32x16_bf16 v[160:175], v[196:199], v[148:151], v[160:175]\n"
    "s_waitcnt lgkmcnt(0)\n"
    "v_mfma_f32_32x32x16_bf16 v[160:175], v[200:203], v[152:155], v[160:175]\n"
    "v_mfma_f32_32x32x16_bf16 v[160:175], v[204:207], v[156:159], v[160:175]\n"
    "s_nop 11\n"
    "v_exp_f32 v160, v160\n"
    "v_exp_f32 v161, v161\n"
    "s_waitcnt lgkmcnt(0)\n"
    "s_barrier\n"
    "ds_read_b128 v[192:195], v244 offset:8192\n"
    "ds_read_b128 a[0:3], v236 offset:0\n"
    "s_waitcnt vmcnt(22)\n"
    "ds_read_b128 v[216:219], v233 offset:16384\n"
    "ds_read_b128 v[220:223], v233 offset:17408\n"
    "v_exp_f32 v162, v162\n"
    "v_add_f32 v240, v160, v161\n"
    "v_cvt_pk_bf16_f32 v208, v160, v161\n"
    "s_add_u32 s58, s56, 49152\n"
    "s_mov_b32 m0, s58\n"
    "s_nop 0\n"
    "ds_read_b128 v[196:199], v245 offset:8192\n"
    "ds_read_b128 a[4:7], v236 offset:1024\n"
    "buffer_load_dwordx4 v232, s[24:27], s58 offen offset:0 nt lds\n"
    "v_exp_f32 v163, v163\n"
    "s_add_u32 m0, s58, 896\n"
    "s_nop 0\n"
    "buffer_load_dwordx4 v232, s[24:27], s58 offen offset:128 nt lds\n"
    "ds_read_b128 v[200:203], v244 offset:8704\n"
    "ds_read_b128 a[8:11], v236 offset:2048\n"
    "s_waitcnt vmcnt(20)\n"
    "v_exp_f32 v164, v164\n"
    "v_add_f32 v241, v162, v163\n"
    "v_cvt_pk_bf16_f32 v209, v162, v163\n"
    "v_perm_b32 v226, v74, v72, s53\n"
    "v_perm_b32 v227, v78, v76, s53\n"
    "v_max3_f32 v234, |v226|, |v227|, v234\n"
    "ds_write_b64 v235, v[226:227] offset:8192\n"
    "ds_read_b128 v[204:207], v245 offset:8704\n"
    "ds_read_b128 a[12:15], v236 offset:3072\n"
    "v_exp_f32 v165, v165\n"
    "v_perm_b32 v228, v74, v72, s54\n"
    "v_perm_b32 v229, v78, v76, s54\n"
    "ds_write_b64 v235, v[228:229] offset:9216\n"
    "v_perm_b32 v226, v75, v73, s53\n"
    "v_perm_b32 v227, v79, v77, s53\n"
    "v_max3_f32 v234, |v226|, |v227|, v234\n"
    "s_waitcnt lgkmcnt(7)\n"
    "v_mfma_f32_32x32x16_bf16 v[176:191], v[192:195], v[128:131], 0\n"
    "ds_read_b128 v[192:195], v244 offset:9216\n"
    "ds_read_b128 a[16:19], v236 offset:4096\n"
    "ds_write_b64 v235, v[226:227] offset:10240\n"
    "v_exp_f32 v166, v166\n"
    "v_cvt_pk_bf16_f32 v210, v164, v165\n"
    "v_perm_b32 v228, v75, v73, s54\n"
    "v_perm_b32 v229, v79, v77, s54\n"
    "v_lshlrev_b32 v224, 16, v216\n"
    "v_mfma_f32_32x32x16_bf16 v[176:191], v[196:199], v[132:135], v[176:191]\n"
    "ds_read_b128 v[196:199], v245 offset:9216\n"
    "ds_read_b128 a[20:23], v236 offset:5120\n"
    "ds_write_b64 v235, v[228:229] offset:11264\n"
    "v_exp_f32 v167, v167\n"
    "v_max3_f32 v234, |v72|, |v73|, v234\n"
    "v_max3_f32 v234, |v74|, |v75|, v234\n"
    "v_max3_f32 v234, |v76|, |v77|, v234\n"
    "s_waitcnt lgkmcnt(8)\n"
    "v_mfma_f32_32x32x16_bf16 v[176:191], v[200:203], v[136:139], v[176:191]\n"
    "ds_read_b128 v[200:203], v244 offset:9728\n"
    "ds_read_b128 a[24:27], v236 offset:6144\n"
    "v_exp_f32 v168, v168\n"
    "v_cvt_pk_bf16_f32 v211, v166, v167\n"
    "v_max3_f32 v234, |v78|, |v79|, v234\n"
    "v_lshlrev_b32 v230, 16, v72\n"
    "v_lshlrev_b32 v231, 16, v73\n"
    "v_max3_f32 v234, |v230|, |v231|, v234\n"
    "v_lshlrev_b32 v230, 16, v76\n"
    "v_mfma_f32_32x32x16_bf16 v[176:191], v[204:207], v[140:143], v[176:191]\n"
    "ds_read_b128 v[204:207], v245 offset:9728\n"
    "ds_read_b128 a[28:31], v236 offset:7168\n"
    "v_exp_f32 v169, v169\n"
    "v_lshlrev_b32 v231, 16, v77\n"
    "v_max3_f32 v234, |v230|, |v231|, v234\n"
    "v_and_b32 v225, 0xffff0000, v216\n"
    "v_mul_f32 v224, s55, v224\n"
    "v_mul_f32 v225, s55, v225\n"
    "s_waitcnt lgkmcnt(6)\n"
    "v_mfma_f32_32x32x16_bf16 v[176:191], v[192:195], v[144:147], v[176:191]\n"
    "v_exp_f32 v170, v170\n"
    "v_cvt_pk_bf16_f32 v216, v224, v225\n"
    "v_lshlrev_b32 v224, 16, v217\n"
    "v_and_b32 v225, 0xffff0000, v217\n"
    "v_mul_f32 v224, s55, v224\n"
    "v_mul_f32 v225, s55, v225\n"
    "v_cvt_pk_bf16_f32 v217, v224, v225\n"
    "v_lshlrev_b32 v224, 16, v218\n"
    "v_and_b32 v225, 0xffff0000, v218\n"
    "v_mfma_f32_32x32x16_bf16 v[176:191], v[196:199], v[148:151], v[176:191]\n"
    "v_exp_f32 v171, v171\n"
    "v_mul_f32 v224, s55, v224\n"
    "v_mul_f32 v225, s55, v225\n"
    "v_cvt_pk_bf16_f32 v218, v224, v225\n"
    "v_lshlrev_b32 v224, 16, v219\n"
    "v_and_b32 v225, 0xffff0000, v219\n"
    "v_mul_f32 v224, s55, v224\n"
    "v_mul_f32 v225, s55, v225\n"
    "v_add_f32 v240, v240, v170\n"
    "s_waitcnt lgkmcnt(1)\n"
    "v_mfma_f32_32x32x16_bf16 v[176:191], v[200:203], v[152:155], v[176:191]\n"
    "v_exp_f32 v172, v172\n"
    "v_cvt_pk_bf16_f32 v219, v224, v225\n"
    "ds_write_b128 v233, v[216:219] offset:16384\n"
    "v_lshlrev_b32 v224, 16, v220\n"
    "v_and_b32 v225, 0xffff0000, v220\n"
    "v_mul_f32 v224, s55, v224\n"
    "v_mul_f32 v225, s55, v225\n"
    "v_cvt_pk_bf16_f32 v220, v224, v225\n"
    "v_add_f32 v241, v241, v171\n"
    "v_mfma_f32_32x32x16_bf16 v[176:191], v[204:207], v[156:159], v[176:191]\n"
    "v_exp_f32 v173, v173\n"
    "v_lshlrev_b32 v224, 16, v221\n"
    "v_and_b32 v225, 0xffff0000, v221\n"
    "v_mul_f32 v224, s55, v224\n"
    "v_mul_f32 v225, s55, v225\n"
    "v_cvt_pk_bf16_f32 v221, v224, v225\n"
    "v_lshlrev_b32 v224, 16, v222\n"
    "v_and_b32 v225, 0xffff0000, v222\n"
    "v_mul_f32 v224, s55, v224\n"
    "v_add_f32 v240, v240, v172\n"
    "v_mfma_f32_32x32x16_bf16 v[0:15], v[208:211], a[0:3], 0\n"
    "v_exp_f32 v174, v174\n"
    "v_mul_f32 v225, s55, v225\n"
    "v_cvt_pk_bf16_f32 v222, v224, v225\n"
    "v_lshlrev_b32 v224, 16, v223\n"
    "v_and_b32 v225, 0xffff0000, v223\n"
    "v_mul_f32 v224, s55, v224\n"
    "v_mul_f32 v225, s55, v225\n"
    "v_cvt_pk_bf16_f32 v223, v224, v225\n"
    "v_cvt_pk_bf16_f32 v212, v168, v169\n"
    "v_add_f32 v241, v241, v173\n"
    "v_mfma_f32_32x32x16_bf16 v[16:31], v[208:211], a[4:7], 0\n"
    "ds_write_b128 v233, v[220:223] offset:17408\n"
    "v_exp_f32 v175, v175\n"
    "v_cvt_pk_bf16_f32 v213, v170, v171\n"
    "v_cvt_pk_bf16_f32 v214, v172, v173\n"
    "s_add_u32 s59, s56, 40960\n"
    "buffer_load_dwordx2 v[104:105], v250, s[28:31], s59 offen offset:0 nt\n"
    "v_add_f32 v240, v240, v174\n"
    "v_mfma_f32_32x32x16_bf16 v[32:47], v[208:211], a[8:11], 0\n"
    "buffer_load_dwordx2 v[106:107], v250, s[28:31], s59 offen offset:256 nt\n"
    "buffer_load_dwordx2 v[108:109], v250, s[28:31], s59 offen offset:512 nt\n"
    "v_exp_f32 v176, v176\n"
    "v_cvt_pk_bf16_f32 v215, v174, v175\n"
    "v_add_f32 v240, v240, v164\n"
    "v_add_f32 v241, v241, v175\n"
    "v_mfma_f32_32x32x16_bf16 v[48:63], v[208:211], a[12:15], 0\n"
    "buffer_load_dwordx2 v[110:111], v250, s[28:31], s59 offen offset:768 nt\n"
    "v_exp_f32 v177, v177\n"
    "v_add_f32 v241, v241, v165\n"
    "v_add_f32 v240, v240, v166\n"
    "v_add_f32 v241, v241, v167\n"
    "v_add_f32 v240, v240, v168\n"
    "v_add_f32 v241, v241, v169\n"
    "s_waitcnt lgkmcnt(0)\n"
    "s_barrier\n"
    "v_mfma_f32_32x32x16_bf16 v[0:15], v[212:215], a[16:19], v[0:15]\n"
    "ds_read_b128 v[192:195], v244 offset:16384\n"
    "ds_read_b128 a[32:35], v236 offset:8192\n"
    "s_waitcnt vmcnt(22)\n"
    "ds_read_b128 v[216:219], v233 offset:24576\n"
    "ds_read_b128 v[220:223], v233 offset:25600\n"
    "v_exp_f32 v178, v178\n"
    "v_cvt_pk_bf16_f32 v208, v176, v177\n"
    "v_add_f32 v240, v240, v176\n"
    "v_add_f32 v241, v241, v177\n"
    "v_mfma_f32_32x32x16_bf16 v[16:31], v[212:215], a[20:23], v[16:31]\n"
    "ds_read_b128 v[196:199], v245 offset:16384\n"
    "ds_read_b128 a[36:39], v236 offset:9216\n"
    "v_exp_f32 v179, v179\n"
    "s_add_u32 s58, s56, 57344\n"
    "s_mov_b32 m0, s58\n"
    "s_nop 0\n"
    "buffer_load_dwordx4 v232, s[24:27], s58 offen offset:0 nt lds\n"
    "v_mfma_f32_32x32x16_bf16 v[32:47], v[212:215], a[24:27], v[32:47]\n"
    "ds_read_b128 v[200:203], v244 offset:16896\n"
    "ds_read_b128 a[40:43], v236 offset:10240\n"
    "s_waitcnt vmcnt(19)\n"
    "v_exp_f32 v180, v180\n"
    "v_cvt_pk_bf16_f32 v209, v178, v179\n"
    "v_perm_b32 v226, v82, v80, s53\n"
    "v_perm_b32 v227, v86, v84, s53\n"
    "v_max3_f32 v234, |v226|, |v227|, v234\n"
    "s_add_u32 m0, s58, 896\n"
    "v_mfma_f32_32x32x16_bf16 v[48:63], v[212:215], a[28:31], v[48:63]\n"
    "ds_read_b128 v[204:207], v245 offset:16896\n"
    "ds_read_b128 a[44:47], v236 offset:11264\n"
    "ds_write_b64 v235, v[226:227] offset:16384\n"
    "v_exp_f32 v181, v181\n"
    "v_perm_b32 v228, v82, v80, s54\n"
    "v_perm_b32 v229, v86, v84, s54\n"
    "s_nop 0\n"
    "v_add_f32 v240, v240, v180\n"
    "s_waitcnt lgkmcnt(6)\n"
    "v_mfma_f32_32x32x16_bf16 v[160:175], v[192:195], v[128:131], 0\n"
    "ds_read_b128 v[192:195], v244 offset:17408\n"
    "ds_read_b128 a[48:51], v236 offset:12288\n"
    "ds_write_b64 v235, v[228:229] offset:17408\n"
    "v_exp_f32 v182, v182\n"
    "v_cvt_pk_bf16_f32 v210, v180, v181\n"
    "v_perm_b32 v226, v83, v81, s53\n"
    "v_perm_b32 v227, v87, v85, s53\n"
    "v_max3_f32 v234, |v226|, |v227|, v234\n"
    "v_add_f32 v241, v241, v181\n"
    "v_mfma_f32_32x32x16_bf16 v[160:175], v[196:199], v[132:135], v[160:175]\n"
    "ds_read_b128 v[196:199], v245 offset:17408\n"
    "ds_read_b128 a[52:55], v236 offset:13312\n"
    "ds_write_b64 v235, v[226:227] offset:18432\n"
    "v_exp_f32 v183, v183\n"
    "v_perm_b32 v228, v83, v81, s54\n"
    "v_perm_b32 v229, v87, v85, s54\n"
    "v_lshlrev_b32 v224, 16, v216\n"
    "v_add_f32 v240, v240, v182\n"
    "s_waitcnt lgkmcnt(8)\n"
    "v_mfma_f32_32x32x16_bf16 v[160:175], v[200:203], v[136:139], v[160:175]\n"
    "ds_read_b128 v[200:203], v244 offset:17920\n"
    "ds_read_b128 a[56:59], v236 offset:14336\n"
    "ds_write_b64 v235, v[228:229] offset:19456\n"
    "v_exp_f32 v184, v184\n"
    "v_cvt_pk_bf16_f32 v211, v182, v183\n"
    "v_max3_f32 v234, |v80|, |v81|, v234\n"
    "v_max3_f32 v234, |v82|, |v83|, v234\n"
    "v_max3_f32 v234, |v84|, |v85|, v234\n"
    "v_add_f32 v241, v241, v183\n"
    "v_mfma_f32_32x32x16_bf16 v[160:175], v[204:207], v[140:143], v[160:175]\n"
    "ds_read_b128 v[204:207], v245 offset:17920\n"
    "ds_read_b128 a[60:63], v236 offset:15360\n"
    "v_exp_f32 v185, v185\n"
    "v_max3_f32 v234, |v86|, |v87|, v234\n"
    "v_lshlrev_b32 v230, 16, v80\n"
    "v_lshlrev_b32 v231, 16, v81\n"
    "v_max3_f32 v234, |v230|, |v231|, v234\n"
    "v_lshlrev_b32 v230, 16, v84\n"
    "v_add_f32 v240, v240, v184\n"
    "s_waitcnt lgkmcnt(7)\n"
    "v_mfma_f32_32x32x16_bf16 v[160:175], v[192:195], v[144:147], v[160:175]\n"
    "v_exp_f32 v186, v186\n"
    "v_lshlrev_b32 v231, 16, v85\n"
    "v_max3_f32 v234, |v230|, |v231|, v234\n"
    "v_and_b32 v225, 0xffff0000, v216\n"
    "v_mul_f32 v224, s55, v224\n"
    "v_mul_f32 v225, s55, v225\n"
    "v_cvt_pk_bf16_f32 v216, v224, v225\n"
    "v_lshlrev_b32 v224, 16, v217\n"
    "v_and_b32 v225, 0xffff0000, v217\n"
    "v_add_f32 v241, v241, v185\n"
    "v_mfma_f32_32x32x16_bf16 v[160:175], v[196:199], v[148:151], v[160:175]\n"
    "v_exp_f32 v187, v187\n"
    "v_mul_f32 v224, s55, v224\n"
    "v_mul_f32 v225, s55, v225\n"
    "v_cvt_pk_bf16_f32 v217, v224, v225\n"
    "v_lshlrev_b32 v224, 16, v218\n"
    "v_and_b32 v225, 0xffff0000, v218\n"
    "v_mul_f32 v224, s55, v224\n"
    "v_mul_f32 v225, s55, v225\n"
    "v_add_f32 v240, v240, v186\n"
    "s_waitcnt lgkmcnt(1)\n"
    "v_mfma_f32_32x32x16_bf16 v[160:175], v[200:203], v[152:155], v[160:175]\n"
    "v_exp_f32 v188, v188\n"
    "v_cvt_pk_bf16_f32 v218, v224, v225\n"
    "v_lshlrev_b32 v224, 16, v219\n"
    "v_and_b32 v225, 0xffff0000, v219\n"
    "v_mul_f32 v224, s55, v224\n"
    "v_mul_f32 v225, s55, v225\n"
    "v_cvt_pk_bf16_f32 v219, v224, v225\n"
    "ds_write_b128 v233, v[216:219] offset:24576\n"
    "v_add_f32 v241, v241, v187\n"
    "v_mfma_f32_32x32x16_bf16 v[160:175], v[204:207], v[156:159], v[160:175]\n"
    "v_exp_f32 v189, v189\n"
    "v_lshlrev_b32 v224, 16, v220\n"
    "v_and_b32 v225, 0xffff0000, v220\n"
    "v_mul_f32 v224, s55, v224\n"
    "v_mul_f32 v225, s55, v225\n"
    "v_cvt_pk_bf16_f32 v220, v224, v225\n"
    "v_lshlrev_b32 v224, 16, v221\n"
    "v_and_b32 v225, 0xffff0000, v221\n"
    "v_mul_f32 v224, s55, v224\n"
    "v_add_f32 v240, v240, v188\n"
    "v_mfma_f32_32x32x16_bf16 v[0:15], v[208:211], a[32:35], v[0:15]\n"
    "v_exp_f32 v190, v190\n"
    "v_mul_f32 v225, s55, v225\n"
    "v_cvt_pk_bf16_f32 v221, v224, v225\n"
    "v_lshlrev_b32 v224, 16, v222\n"
    "v_and_b32 v225, 0xffff0000, v222\n"
    "v_mul_f32 v224, s55, v224\n"
    "v_mul_f32 v225, s55, v225\n"
    "v_cvt_pk_bf16_f32 v222, v224, v225\n"
    "v_lshlrev_b32 v224, 16, v223\n"
    "v_add_f32 v241, v241, v189\n"
    "v_mfma_f32_32x32x16_bf16 v[16:31], v[208:211], a[36:39], v[16:31]\n"
    "v_exp_f32 v191, v191\n"
    "v_and_b32 v225, 0xffff0000, v223\n"
    "v_mul_f32 v224, s55, v224\n"
    "v_mul_f32 v225, s55, v225\n"
    "v_cvt_pk_bf16_f32 v223, v224, v225\n"
    "ds_write_b128 v233, v[220:223] offset:25600\n"
    "v_cvt_pk_bf16_f32 v212, v184, v185\n"
    "v_cvt_pk_bf16_f32 v213, v186, v187\n"
    "v_add_f32 v240, v240, v190\n"
    "v_mfma_f32_32x32x16_bf16 v[32:47], v[208:211], a[40:43], v[32:47]\n"
    "buffer_load_dwordx4 v232, s[24:27], s58 offen offset:128 nt lds\n"
    "v_exp_f32 v160, v160\n"
    "v_cvt_pk_bf16_f32 v215, v190, v191\n"
    "v_cvt_pk_bf16_f32 v214, v188, v189\n"
    "s_add_u32 s59, s56, 49152\n"
    "v_add_f32 v240, v240, v178\n"
    "v_add_f32 v241, v241, v179\n"
    "v_add_f32 v241, v241, v191\n"
    "v_mfma_f32_32x32x16_bf16 v[48:63], v[208:211], a[44:47], v[48:63]\n"
    "buffer_load_dwordx2 v[112:113], v250, s[28:31], s59 offen offset:0 nt\n"
    "buffer_load_dwordx2 v[114:115], v250, s[28:31], s59 offen offset:256 nt\n"
    "buffer_load_dwordx2 v[116:117], v250, s[28:31], s59 offen offset:512 nt\n"
    "buffer_load_dwordx2 v[118:119], v250, s[28:31], s59 offen offset:768 nt\n"
    "v_exp_f32 v161, v161\n"
    "s_waitcnt lgkmcnt(0)\n"
    "s_barrier\n"
    "v_mfma_f32_32x32x16_bf16 v[0:15], v[212:215], a[48:51], v[0:15]\n"
    "ds_read_b128 v[192:195], v244 offset:24576\n"
    "ds_read_b128 a[64:67], v236 offset:16384\n"
    "s_waitcnt vmcnt(22)\n"
    "ds_read_b128 v[216:219], v233 offset:32768\n"
    "ds_read_b128 v[220:223], v233 offset:33792\n"
    "v_exp_f32 v162, v162\n"
    "v_cvt_pk_bf16_f32 v208, v160, v161\n"
    "v_add_f32 v240, v240, v160\n"
    "v_add_f32 v241, v241, v161\n"
    "v_mfma_f32_32x32x16_bf16 v[16:31], v[212:215], a[52:55], v[16:31]\n"
    "ds_read_b128 v[196:199], v245 offset:24576\n"
    "ds_read_b128 a[68:71], v236 offset:17408\n"
    "v_exp_f32 v163, v163\n"
    "s_add_u32 s59, s56, 57344\n"
    "buffer_load_dwordx2 v[120:121], v250, s[28:31], s59 offen offset:0 nt\n"
    "v_add_f32 v240, v240, v162\n"
    "v_mfma_f32_32x32x16_bf16 v[32:47], v[212:215], a[56:59], v[32:47]\n"
    "ds_read_b128 v[200:203], v244 offset:25088\n"
    "ds_read_b128 a[72:75], v236 offset:18432\n"
    "s_waitcnt vmcnt(19)\n"
    "v_exp_f32 v164, v164\n"
    "v_cvt_pk_bf16_f32 v209, v162, v163\n"
    "v_perm_b32 v226, v90, v88, s53\n"
    "v_perm_b32 v227, v94, v92, s53\n"
    "v_max3_f32 v234, |v226|, |v227|, v234\n"
    "v_add_f32 v241, v241, v163\n"
    "v_mfma_f32_32x32x16_bf16 v[48:63], v[212:215], a[60:63], v[48:63]\n"
    "ds_read_b128 v[204:207], v245 offset:25088\n"
    "ds_read_b128 a[76:79], v236 offset:19456\n"
    "ds_write_b64 v235, v[226:227] offset:24576\n"
    "v_exp_f32 v165, v165\n"
    "v_perm_b32 v228, v90, v88, s54\n"
    "v_perm_b32 v229, v94, v92, s54\n"
    "v_add_f32 v240, v240, v164\n"
    "s_waitcnt lgkmcnt(6)\n"
    "v_mfma_f32_32x32x16_bf16 v[176:191], v[192:195], v[128:131], 0\n"
    "ds_read_b128 v[192:195], v244 offset:25600\n"
    "ds_read_b128 a[80:83], v236 offset:20480\n"
    "ds_write_b64 v235, v[228:229] offset:25600\n"
    "v_exp_f32 v166, v166\n"
    "v_cvt_pk_bf16_f32 v210, v164, v165\n"
    "v_perm_b32 v226, v91, v89, s53\n"
    "v_perm_b32 v227, v95, v93, s53\n"
    "v_max3_f32 v234, |v226|, |v227|, v234\n"
    "v_mfma_f32_32x32x16_bf16 v[176:191], v[196:199], v[132:135], v[176:191]\n"
    "ds_read_b128 v[196:199], v245 offset:25600\n"
    "ds_read_b128 a[84:87], v236 offset:21504\n"
    "ds_write_b64 v235, v[226:227] offset:26624\n"
    "v_exp_f32 v167, v167\n"
    "v_perm_b32 v228, v91, v89, s54\n"
    "v_perm_b32 v229, v95, v93, s54\n"
    "v_lshlrev_b32 v224, 16, v216\n"
    "s_waitcnt lgkmcnt(8)\n"
    "v_mfma_f32_32x32x16_bf16 v[176:191], v[200:203], v[136:139], v[176:191]\n"
    "ds_read_b128 v[200:203], v244 offset:26112\n"
    "ds_read_b128 a[88:91], v236 offset:22528\n"
    "ds_write_b64 v235, v[228:229] offset:27648\n"
    "v_exp_f32 v168, v168\n"
    "v_cvt_pk_bf16_f32 v211, v166, v167\n"
    "v_max3_f32 v234, |v88|, |v89|, v234\n"
    "v_max3_f32 v234, |v90|, |v91|, v234\n"
    "v_max3_f32 v234, |v92|, |v93|, v234\n"
    "v_mfma_f32_32x32x16_bf16 v[176:191], v[204:207], v[140:143], v[176:191]\n"
    "ds_read_b128 v[204:207], v245 offset:26112\n"
    "ds_read_b128 a[92:95], v236 offset:23552\n"
    "v_exp_f32 v169, v169\n"
    "v_max3_f32 v234, |v94|, |v95|, v234\n"
    "v_lshlrev_b32 v230, 16, v88\n"
    "v_lshlrev_b32 v231, 16, v89\n"
    "v_max3_f32 v234, |v230|, |v231|, v234\n"
    "v_lshlrev_b32 v230, 16, v92\n"
    "s_waitcnt lgkmcnt(7)\n"
    "v_mfma_f32_32x32x16_bf16 v[176:191], v[192:195], v[144:147], v[176:191]\n"
    "v_exp_f32 v170, v170\n"
    "v_lshlrev_b32 v231, 16, v93\n"
    "v_max3_f32 v234, |v230|, |v231|, v234\n"
    "v_and_b32 v225, 0xffff0000, v216\n"
    "v_mul_f32 v224, s55, v224\n"
    "v_mul_f32 v225, s55, v225\n"
    "v_cvt_pk_bf16_f32 v216, v224, v225\n"
    "v_lshlrev_b32 v224, 16, v217\n"
    "v_and_b32 v225, 0xffff0000, v217\n"
    "v_mfma_f32_32x32x16_bf16 v[176:191], v[196:199], v[148:151], v[176:191]\n"
    "v_exp_f32 v171, v171\n"
    "v_mul_f32 v224, s55, v224\n"
    "v_mul_f32 v225, s55, v225\n"
    "v_cvt_pk_bf16_f32 v217, v224, v225\n"
    "v_lshlrev_b32 v224, 16, v218\n"
    "v_and_b32 v225, 0xffff0000, v218\n"
    "v_mul_f32 v224, s55, v224\n"
    "v_mul_f32 v225, s55, v225\n"
    "v_add_f32 v240, v240, v170\n"
    "s_waitcnt lgkmcnt(1)\n"
    "v_mfma_f32_32x32x16_bf16 v[176:191], v[200:203], v[152:155], v[176:191]\n"
    "v_exp_f32 v172, v172\n"
    "v_cvt_pk_bf16_f32 v218, v224, v225\n"
    "v_lshlrev_b32 v224, 16, v219\n"
    "v_and_b32 v225, 0xffff0000, v219\n"
    "v_mul_f32 v224, s55, v224\n"
    "v_mul_f32 v225, s55, v225\n"
    "v_cvt_pk_bf16_f32 v219, v224, v225\n"
    "ds_write_b128 v233, v[216:219] offset:32768\n"
    "v_add_f32 v241, v241, v171\n"
    "v_mfma_f32_32x32x16_bf16 v[176:191], v[204:207], v[156:159], v[176:191]\n"
    "v_exp_f32 v173, v173\n"
    "v_lshlrev_b32 v224, 16, v220\n"
    "v_and_b32 v225, 0xffff0000, v220\n"
    "v_mul_f32 v224, s55, v224\n"
    "v_mul_f32 v225, s55, v225\n"
    "v_cvt_pk_bf16_f32 v220, v224, v225\n"
    "v_lshlrev_b32 v224, 16, v221\n"
    "v_and_b32 v225, 0xffff0000, v221\n"
    "v_mul_f32 v224, s55, v224\n"
    "v_add_f32 v240, v240, v172\n"
    "v_mfma_f32_32x32x16_bf16 v[0:15], v[208:211], a[64:67], v[0:15]\n"
    "v_exp_f32 v174, v174\n"
    "v_mul_f32 v225, s55, v225\n"
    "v_cvt_pk_bf16_f32 v221, v224, v225\n"
    "v_lshlrev_b32 v224, 16, v222\n"
    "v_and_b32 v225, 0xffff0000, v222\n"
    "v_mul_f32 v224, s55, v224\n"
    "v_mul_f32 v225, s55, v225\n"
    "v_cvt_pk_bf16_f32 v222, v224, v225\n"
    "v_lshlrev_b32 v224, 16, v223\n"
    "v_add_f32 v241, v241, v173\n"
    "v_mfma_f32_32x32x16_bf16 v[16:31], v[208:211], a[68:71], v[16:31]\n"
    "v_exp_f32 v175, v175\n"
    "v_and_b32 v225, 0xffff0000, v223\n"
    "v_mul_f32 v224, s55, v224\n"
    "v_mul_f32 v225, s55, v225\n"
    "v_cvt_pk_bf16_f32 v223, v224, v225\n"
    "ds_write_b128 v233, v[220:223] offset:33792\n"
    "v_cvt_pk_bf16_f32 v212, v168, v169\n"
    "v_cvt_pk_bf16_f32 v213, v170, v171\n"
    "v_add_f32 v240, v240, v174\n"
    "v_mfma_f32_32x32x16_bf16 v[32:47], v[208:211], a[72:75], v[32:47]\n"
    "buffer_load_dwordx2 v[122:123], v250, s[28:31], s59 offen offset:256 nt\n"
    "buffer_load_dwordx2 v[124:125], v250, s[28:31], s59 offen offset:512 nt\n"
    "v_exp_f32 v176, v176\n"
    "v_cvt_pk_bf16_f32 v215, v174, v175\n"
    "v_cvt_pk_bf16_f32 v214, v172, v173\n"
    "v_add_f32 v241, v241, v175\n"
    "v_mfma_f32_32x32x16_bf16 v[48:63], v[208:211], a[76:79], v[48:63]\n"
    "buffer_load_dwordx2 v[126:127], v250, s[28:31], s59 offen offset:768 nt\n"
    "v_exp_f32 v177, v177\n"
    "v_add_f32 v241, v241, v165\n"
    "v_add_f32 v240, v240, v166\n"
    "v_add_f32 v241, v241, v167\n"
    "v_add_f32 v240, v240, v168\n"
    "v_add_f32 v241, v241, v169\n"
    "s_waitcnt lgkmcnt(0)\n"
    "s_barrier\n"
    "v_mfma_f32_32x32x16_bf16 v[0:15], v[212:215], a[80:83], v[0:15]\n"
    "ds_read_b128 v[192:195], v244 offset:32768\n"
    "ds_read_b128 a[96:99], v236 offset:24576\n"
    "s_waitcnt vmcnt(20)\n"
    "ds_read_b128 v[216:219], v233 offset:40960\n"
    "ds_read_b128 v[220:223], v233 offset:41984\n"
    "v_exp_f32 v178, v178\n"
    "v_cvt_pk_bf16_f32 v208, v176, v177\n"
    "v_add_f32 v240, v240, v176\n"
    "v_add_f32 v241, v241, v177\n"
    "v_mfma_f32_32x32x16_bf16 v[16:31], v[212:215], a[84:87], v[16:31]\n"
    "ds_read_b128 v[196:199], v245 offset:32768\n"
    "ds_read_b128 a[100:103], v236 offset:25600\n"
    "v_exp_f32 v179, v179\n"
    "s_add_u32 s18, s37, 32768\n"
    "s_add_u32 s19, s18, 4096\n"
    "s_add_u32 m0, s40, 32768\n"
    "s_nop 0\n"
    "v_add_f32 v240, v240, v178\n"
    "v_mfma_f32_32x32x16_bf16 v[32:47], v[212:215], a[88:91], v[32:47]\n"
    "ds_read_b128 v[200:203], v244 offset:33280\n"
    "ds_read_b128 a[104:107], v236 offset:26624\n"
    "s_waitcnt vmcnt(16)\n"
    "v_exp_f32 v180, v180\n"
    "v_cvt_pk_bf16_f32 v209, v178, v179\n"
    "v_perm_b32 v226, v98, v96, s53\n"
    "v_perm_b32 v227, v102, v100, s53\n"
    "v_max3_f32 v234, |v226|, |v227|, v234\n"
    "v_add_f32 v241, v241, v179\n"
    "v_mfma_f32_32x32x16_bf16 v[48:63], v[212:215], a[92:95], v[48:63]\n"
    "ds_read_b128 v[204:207], v245 offset:33280\n"
    "ds_read_b128 a[108:111], v236 offset:27648\n"
    "ds_write_b64 v235, v[226:227] offset:0\n"
    "v_exp_f32 v181, v181\n"
    "v_perm_b32 v228, v98, v96, s54\n"
    "v_perm_b32 v229, v102, v100, s54\n"
    "v_add_f32 v240, v240, v180\n"
    "s_waitcnt lgkmcnt(6)\n"
    "v_mfma_f32_32x32x16_bf16 v[160:175], v[192:195], v[128:131], 0\n"
    "ds_read_b128 v[192:195], v244 offset:33792\n"
    "ds_read_b128 a[112:115], v236 offset:28672\n"
    "ds_write_b64 v235, v[228:229] offset:1024\n"
    "v_exp_f32 v182, v182\n"
    "v_cvt_pk_bf16_f32 v210, v180, v181\n"
    "v_perm_b32 v226, v99, v97, s53\n"
    "v_perm_b32 v227, v103, v101, s53\n"
    "v_max3_f32 v234, |v226|, |v227|, v234\n"
    "v_add_f32 v241, v241, v181\n"
    "v_mfma_f32_32x32x16_bf16 v[160:175], v[196:199], v[132:135], v[160:175]\n"
    "ds_read_b128 v[196:199], v245 offset:33792\n"
    "ds_read_b128 a[116:119], v236 offset:29696\n"
    "ds_write_b64 v235, v[226:227] offset:2048\n"
    "v_exp_f32 v183, v183\n"
    "v_perm_b32 v228, v99, v97, s54\n"
    "v_perm_b32 v229, v103, v101, s54\n"
    "v_lshlrev_b32 v224, 16, v216\n"
    "v_add_f32 v240, v240, v182\n"
    "s_waitcnt lgkmcnt(8)\n"
    "v_mfma_f32_32x32x16_bf16 v[160:175], v[200:203], v[136:139], v[160:175]\n"
    "ds_read_b128 v[200:203], v244 offset:34304\n"
    "ds_read_b128 a[120:123], v236 offset:30720\n"
    "ds_write_b64 v235, v[228:229] offset:3072\n"
    "v_exp_f32 v184, v184\n"
    "v_cvt_pk_bf16_f32 v211, v182, v183\n"
    "v_max3_f32 v234, |v96|, |v97|, v234\n"
    "v_max3_f32 v234, |v98|, |v99|, v234\n"
    "v_max3_f32 v234, |v100|, |v101|, v234\n"
    "v_add_f32 v241, v241, v183\n"
    "v_mfma_f32_32x32x16_bf16 v[160:175], v[204:207], v[140:143], v[160:175]\n"
    "ds_read_b128 v[204:207], v245 offset:34304\n"
    "ds_read_b128 a[124:127], v236 offset:31744\n"
    "v_exp_f32 v185, v185\n"
    "v_max3_f32 v234, |v102|, |v103|, v234\n"
    "v_lshlrev_b32 v230, 16, v96\n"
    "v_lshlrev_b32 v231, 16, v97\n"
    "v_max3_f32 v234, |v230|, |v231|, v234\n"
    "v_lshlrev_b32 v230, 16, v100\n"
    "v_add_f32 v240, v240, v184\n"
    "s_waitcnt lgkmcnt(7)\n"
    "v_mfma_f32_32x32x16_bf16 v[160:175], v[192:195], v[144:147], v[160:175]\n"
    "v_exp_f32 v186, v186\n"
    "v_lshlrev_b32 v231, 16, v101\n"
    "v_max3_f32 v234, |v230|, |v231|, v234\n"
    "v_and_b32 v225, 0xffff0000, v216\n"
    "v_mul_f32 v224, s55, v224\n"
    "v_mul_f32 v225, s55, v225\n"
    "v_cvt_pk_bf16_f32 v216, v224, v225\n"
    "v_lshlrev_b32 v224, 16, v217\n"
    "v_and_b32 v225, 0xffff0000, v217\n"
    "v_add_f32 v241, v241, v185\n"
    "v_mfma_f32_32x32x16_bf16 v[160:175], v[196:199], v[148:151], v[160:175]\n"
    "v_exp_f32 v187, v187\n"
    "v_mul_f32 v224, s55, v224\n"
    "v_mul_f32 v225, s55, v225\n"
    "v_cvt_pk_bf16_f32 v217, v224, v225\n"
    "v_lshlrev_b32 v224, 16, v218\n"
    "v_and_b32 v225, 0xffff0000, v218\n"
    "v_mul_f32 v224, s55, v224\n"
    "v_mul_f32 v225, s55, v225\n"
    "v_add_f32 v240, v240, v186\n"
    "s_waitcnt lgkmcnt(1)\n"
    "v_mfma_f32_32x32x16_bf16 v[160:175], v[200:203], v[152:155], v[160:175]\n"
    "v_exp_f32 v188, v188\n"
    "v_cvt_pk_bf16_f32 v218, v224, v225\n"
    "v_lshlrev_b32 v224, 16, v219\n"
    "v_and_b32 v225, 0xffff0000, v219\n"
    "v_mul_f32 v224, s55, v224\n"
    "v_mul_f32 v225, s55, v225\n"
    "v_cvt_pk_bf16_f32 v219, v224, v225\n"
    "ds_write_b128 v233, v[216:219] offset:40960\n"
    "v_add_f32 v241, v241, v187\n"
    "v_mfma_f32_32x32x16_bf16 v[160:175], v[204:207], v[156:159], v[160:175]\n"
    "v_exp_f32 v189, v189\n"
    "v_lshlrev_b32 v224, 16, v220\n"
    "v_and_b32 v225, 0xffff0000, v220\n"
    "v_mul_f32 v224, s55, v224\n"
    "v_mul_f32 v225, s55, v225\n"
    "v_cvt_pk_bf16_f32 v220, v224, v225\n"
    "v_lshlrev_b32 v224, 16, v221\n"
    "v_and_b32 v225, 0xffff0000, v221\n"
    "v_mul_f32 v224, s55, v224\n"
    "v_add_f32 v240, v240, v188\n"
    "v_mfma_f32_32x32x16_bf16 v[0:15], v[208:211], a[96:99], v[0:15]\n"
    "v_exp_f32 v190, v190\n"
    "v_mul_f32 v225, s55, v225\n"
    "v_cvt_pk_bf16_f32 v221, v224, v225\n"
    "v_lshlrev_b32 v224, 16, v222\n"
    "v_and_b32 v225, 0xffff0000, v222\n"
    "v_mul_f32 v224, s55, v224\n"
    "v_mul_f32 v225, s55, v225\n"
    "v_cvt_pk_bf16_f32 v222, v224, v225\n"
    "v_lshlrev_b32 v224, 16, v223\n"
    "v_add_f32 v241, v241, v189\n"
    "v_mfma_f32_32x32x16_bf16 v[16:31], v[208:211], a[100:103], v[16:31]\n"
    "v_exp_f32 v191, v191\n"
    "v_and_b32 v225, 0xffff0000, v223\n"
    "v_mul_f32 v224, s55, v224\n"
    "v_mul_f32 v225, s55, v225\n"
    "v_cvt_pk_bf16_f32 v223, v224, v225\n"
    "ds_write_b128 v233, v[220:223] offset:41984\n"
    "v_cvt_pk_bf16_f32 v212, v184, v185\n"
    "v_cvt_pk_bf16_f32 v213, v186, v187\n"
    "v_add_f32 v240, v240, v190\n"
    "v_mfma_f32_32x32x16_bf16 v[32:47], v[208:211], a[104:107], v[32:47]\n"
    "buffer_load_dwordx4 v248, s[20:23], s18 offen offset:0 nt lds\n"
    "buffer_load_dwordx4 v249, s[20:23], s18 offen offset:2048 nt lds\n"
    "v_exp_f32 v160, v160\n"
    "v_cvt_pk_bf16_f32 v215, v190, v191\n"
    "v_cvt_pk_bf16_f32 v214, v188, v189\n"
    "v_add_f32 v241, v241, v191\n"
    "v_mfma_f32_32x32x16_bf16 v[48:63], v[208:211], a[108:111], v[48:63]\n"
    "v_exp_f32 v161, v161\n"
    "s_add_u32 m0, s40, 33664\n"
    "s_nop 0\n"
    "buffer_load_dwordx4 v248, s[20:23], s18 offen offset:128 nt lds\n"
    "buffer_load_dwordx4 v249, s[20:23], s18 offen offset:2176 nt lds\n"
    "s_add_u32 m0, s40, 36864\n"
    "s_nop 0\n"
    "buffer_load_dwordx4 v248, s[20:23], s19 offen offset:0 nt lds\n"
    "buffer_load_dwordx4 v249, s[20:23], s19 offen offset:2048 nt lds\n"
    "s_add_u32 m0, s40, 37760\n"
    "s_nop 0\n"
    "buffer_load_dwordx4 v248, s[20:23], s19 offen offset:128 nt lds\n"
    "buffer_load_dwordx4 v249, s[20:23], s19 offen offset:2176 nt lds\n"
    "s_waitcnt lgkmcnt(0)\n"
    "s_barrier\n"
    "v_mfma_f32_32x32x16_bf16 v[0:15], v[212:215], a[112:115], v[0:15]\n"
    "ds_read_b128 v[192:195], v244 offset:40960\n"
    "ds_read_b128 a[128:131], v236 offset:0\n"
    "s_waitcnt vmcnt(22)\n"
    "ds_read_b128 v[216:219], v233 offset:49152\n"
    "ds_read_b128 v[220:223], v233 offset:50176\n"
    "v_exp_f32 v162, v162\n"
    "v_cvt_pk_bf16_f32 v208, v160, v161\n"
    "v_add_f32 v240, v240, v160\n"
    "v_add_f32 v241, v241, v161\n"
    "v_mfma_f32_32x32x16_bf16 v[16:31], v[212:215], a[116:119], v[16:31]\n"
    "ds_read_b128 v[196:199], v245 offset:40960\n"
    "ds_read_b128 a[132:135], v236 offset:1024\n"
    "v_exp_f32 v163, v163\n"
    "v_add_f32 v240, v240, v162\n"
    "v_mfma_f32_32x32x16_bf16 v[32:47], v[212:215], a[120:123], v[32:47]\n"
    "ds_read_b128 v[200:203], v244 offset:41472\n"
    "ds_read_b128 a[136:139], v236 offset:2048\n"
    "s_waitcnt vmcnt(18)\n"
    "v_exp_f32 v164, v164\n"
    "v_cvt_pk_bf16_f32 v209, v162, v163\n"
    "v_perm_b32 v226, v106, v104, s53\n"
    "v_perm_b32 v227, v110, v108, s53\n"
    "v_max3_f32 v234, |v226|, |v227|, v234\n"
    "v_add_f32 v241, v241, v163\n"
    "v_mfma_f32_32x32x16_bf16 v[48:63], v[212:215], a[124:127], v[48:63]\n"
    "ds_read_b128 v[204:207], v245 offset:41472\n"
    "ds_read_b128 a[140:143], v236 offset:3072\n"
    "ds_write_b64 v235, v[226:227] offset:8192\n"
    "v_exp_f32 v165, v165\n"
    "v_perm_b32 v228, v106, v104, s54\n"
    "v_perm_b32 v229, v110, v108, s54\n"
    "v_add_f32 v240, v240, v164\n"
    "s_waitcnt lgkmcnt(6)\n"
    "v_mfma_f32_32x32x16_bf16 v[176:191], v[192:195], v[128:131], 0\n"
    "ds_read_b128 v[192:195], v244 offset:41984\n"
    "ds_read_b128 a[144:147], v236 offset:4096\n"
    "ds_write_b64 v235, v[228:229] offset:9216\n"
    "v_exp_f32 v166, v166\n"
    "v_cvt_pk_bf16_f32 v210, v164, v165\n"
    "v_perm_b32 v226, v107, v105, s53\n"
    "v_perm_b32 v227, v111, v109, s53\n"
    "v_max3_f32 v234, |v226|, |v227|, v234\n"
    "v_mfma_f32_32x32x16_bf16 v[176:191], v[196:199], v[132:135], v[176:191]\n"
    "ds_read_b128 v[196:199], v245 offset:41984\n"
    "ds_read_b128 a[148:151], v236 offset:5120\n"
    "ds_write_b64 v235, v[226:227] offset:10240\n"
    "v_exp_f32 v167, v167\n"
    "v_perm_b32 v228, v107, v105, s54\n"
    "v_perm_b32 v229, v111, v109, s54\n"
    "v_lshlrev_b32 v224, 16, v216\n"
    "s_waitcnt lgkmcnt(8)\n"
    "v_mfma_f32_32x32x16_bf16 v[176:191], v[200:203], v[136:139], v[176:191]\n"
    "ds_read_b128 v[200:203], v244 offset:42496\n"
    "ds_read_b128 a[152:155], v236 offset:6144\n"
    "ds_write_b64 v235, v[228:229] offset:11264\n"
    "v_exp_f32 v168, v168\n"
    "v_cvt_pk_bf16_f32 v211, v166, v167\n"
    "v_max3_f32 v234, |v104|, |v105|, v234\n"
    "v_max3_f32 v234, |v106|, |v107|, v234\n"
    "v_max3_f32 v234, |v108|, |v109|, v234\n"
    "v_mfma_f32_32x32x16_bf16 v[176:191], v[204:207], v[140:143], v[176:191]\n"
    "ds_read_b128 v[204:207], v245 offset:42496\n"
    "ds_read_b128 a[156:159], v236 offset:7168\n"
    "v_exp_f32 v169, v169\n"
    "v_max3_f32 v234, |v110|, |v111|, v234\n"
    "v_lshlrev_b32 v230, 16, v104\n"
    "v_lshlrev_b32 v231, 16, v105\n"
    "v_max3_f32 v234, |v230|, |v231|, v234\n"
    "v_lshlrev_b32 v230, 16, v108\n"
    "s_waitcnt lgkmcnt(7)\n"
    "v_mfma_f32_32x32x16_bf16 v[176:191], v[192:195], v[144:147], v[176:191]\n"
    "v_exp_f32 v170, v170\n"
    "v_lshlrev_b32 v231, 16, v109\n"
    "v_max3_f32 v234, |v230|, |v231|, v234\n"
    "v_and_b32 v225, 0xffff0000, v216\n"
    "v_mul_f32 v224, s55, v224\n"
    "v_mul_f32 v225, s55, v225\n"
    "v_cvt_pk_bf16_f32 v216, v224, v225\n"
    "v_lshlrev_b32 v224, 16, v217\n"
    "v_and_b32 v225, 0xffff0000, v217\n"
    "v_mfma_f32_32x32x16_bf16 v[176:191], v[196:199], v[148:151], v[176:191]\n"
    "v_exp_f32 v171, v171\n"
    "v_mul_f32 v224, s55, v224\n"
    "v_mul_f32 v225, s55, v225\n"
    "v_cvt_pk_bf16_f32 v217, v224, v225\n"
    "v_lshlrev_b32 v224, 16, v218\n"
    "v_and_b32 v225, 0xffff0000, v218\n"
    "v_mul_f32 v224, s55, v224\n"
    "v_mul_f32 v225, s55, v225\n"
    "s_waitcnt lgkmcnt(1)\n"
    "v_mfma_f32_32x32x16_bf16 v[176:191], v[200:203], v[152:155], v[176:191]\n"
    "v_exp_f32 v172, v172\n"
    "v_cvt_pk_bf16_f32 v218, v224, v225\n"
    "v_lshlrev_b32 v224, 16, v219\n"
    "v_and_b32 v225, 0xffff0000, v219\n"
    "v_mul_f32 v224, s55, v224\n"
    "v_mul_f32 v225, s55, v225\n"
    "v_cvt_pk_bf16_f32 v219, v224, v225\n"
    "ds_write_b128 v233, v[216:219] offset:49152\n"
    "v_mfma_f32_32x32x16_bf16 v[176:191], v[204:207], v[156:159], v[176:191]\n"
    "v_exp_f32 v173, v173\n"
    "v_lshlrev_b32 v224, 16, v220\n"
    "v_and_b32 v225, 0xffff0000, v220\n"
    "v_mul_f32 v224, s55, v224\n"
    "v_mul_f32 v225, s55, v225\n"
    "v_cvt_pk_bf16_f32 v220, v224, v225\n"
    "v_lshlrev_b32 v224, 16, v221\n"
    "v_and_b32 v225, 0xffff0000, v221\n"
    "v_mul_f32 v224, s55, v224\n"
    "v_mfma_f32_32x32x16_bf16 v[0:15], v[208:211], a[128:131], v[0:15]\n"
    "v_exp_f32 v174, v174\n"
    "v_mul_f32 v225, s55, v225\n"
    "v_cvt_pk_bf16_f32 v221, v224, v225\n"
    "v_lshlrev_b32 v224, 16, v222\n"
    "v_and_b32 v225, 0xffff0000, v222\n"
    "v_mul_f32 v224, s55, v224\n"
    "v_mul_f32 v225, s55, v225\n"
    "v_cvt_pk_bf16_f32 v222, v224, v225\n"
    "v_lshlrev_b32 v224, 16, v223\n"
    "v_mfma_f32_32x32x16_bf16 v[16:31], v[208:211], a[132:135], v[16:31]\n"
    "v_exp_f32 v175, v175\n"
    "v_and_b32 v225, 0xffff0000, v223\n"
    "v_mul_f32 v224, s55, v224\n"
    "v_mul_f32 v225, s55, v225\n"
    "v_cvt_pk_bf16_f32 v223, v224, v225\n"
    "ds_write_b128 v233, v[220:223] offset:50176\n"
    "v_cvt_pk_bf16_f32 v212, v168, v169\n"
    "v_cvt_pk_bf16_f32 v213, v170, v171\n"
    "v_mfma_f32_32x32x16_bf16 v[32:47], v[208:211], a[136:139], v[32:47]\n"
    "v_exp_f32 v176, v176\n"
    "v_cvt_pk_bf16_f32 v215, v174, v175\n"
    "v_cvt_pk_bf16_f32 v214, v172, v173\n"
    "v_add_f32 v241, v241, v165\n"
    "v_add_f32 v240, v240, v166\n"
    "v_add_f32 v241, v241, v167\n"
    "v_add_f32 v240, v240, v168\n"
    "v_add_f32 v241, v241, v169\n"
    "v_add_f32 v240, v240, v170\n"
    "v_mfma_f32_32x32x16_bf16 v[48:63], v[208:211], a[140:143], v[48:63]\n"
    "v_exp_f32 v177, v177\n"
    "v_add_f32 v241, v241, v171\n"
    "v_add_f32 v240, v240, v172\n"
    "v_add_f32 v241, v241, v173\n"
    "v_add_f32 v240, v240, v174\n"
    "v_add_f32 v241, v241, v175\n"
    "s_waitcnt lgkmcnt(0)\n"
    "s_barrier\n"
    "v_mfma_f32_32x32x16_bf16 v[0:15], v[212:215], a[144:147], v[0:15]\n"
    "ds_read_b128 v[192:195], v244 offset:49152\n"
    "ds_read_b128 a[160:163], v236 offset:8192\n"
    "s_waitcnt vmcnt(16)\n"
    "ds_read_b128 v[216:219], v233 offset:57344\n"
    "ds_read_b128 v[220:223], v233 offset:58368\n"
    "v_exp_f32 v178, v178\n"
    "v_cvt_pk_bf16_f32 v208, v176, v177\n"
    "v_add_f32 v240, v240, v176\n"
    "v_add_f32 v241, v241, v177\n"
    "v_mfma_f32_32x32x16_bf16 v[16:31], v[212:215], a[148:151], v[16:31]\n"
    "ds_read_b128 v[196:199], v245 offset:49152\n"
    "ds_read_b128 a[164:167], v236 offset:9216\n"
    "v_exp_f32 v179, v179\n"
    "s_add_u32 s18, s37, 65536\n"
    "s_add_u32 s19, s18, 4096\n"
    "s_add_u32 m0, s40, 0\n"
    "s_nop 0\n"
    "v_add_f32 v240, v240, v178\n"
    "v_mfma_f32_32x32x16_bf16 v[32:47], v[212:215], a[152:155], v[32:47]\n"
    "ds_read_b128 v[200:203], v244 offset:49664\n"
    "ds_read_b128 a[168:171], v236 offset:10240\n"
    "s_waitcnt vmcnt(12)\n"
    "v_exp_f32 v180, v180\n"
    "v_cvt_pk_bf16_f32 v209, v178, v179\n"
    "v_perm_b32 v226, v114, v112, s53\n"
    "v_perm_b32 v227, v118, v116, s53\n"
    "v_max3_f32 v234, |v226|, |v227|, v234\n"
    "v_add_f32 v241, v241, v179\n"
    "v_mfma_f32_32x32x16_bf16 v[48:63], v[212:215], a[156:159], v[48:63]\n"
    "ds_read_b128 v[204:207], v245 offset:49664\n"
    "ds_read_b128 a[172:175], v236 offset:11264\n"
    "ds_write_b64 v235, v[226:227] offset:16384\n"
    "v_exp_f32 v181, v181\n"
    "v_perm_b32 v228, v114, v112, s54\n"
    "v_perm_b32 v229, v118, v116, s54\n"
    "v_add_f32 v240, v240, v180\n"
    "s_waitcnt lgkmcnt(6)\n"
    "v_mfma_f32_32x32x16_bf16 v[160:175], v[192:195], v[128:131], 0\n"
    "ds_read_b128 v[192:195], v244 offset:50176\n"
    "ds_read_b128 a[176:179], v236 offset:12288\n"
    "ds_write_b64 v235, v[228:229] offset:17408\n"
    "v_exp_f32 v182, v182\n"
    "v_cvt_pk_bf16_f32 v210, v180, v181\n"
    "v_perm_b32 v226, v115, v113, s53\n"
    "v_perm_b32 v227, v119, v117, s53\n"
    "v_max3_f32 v234, |v226|, |v227|, v234\n"
    "v_add_f32 v241, v241, v181\n"
    "v_mfma_f32_32x32x16_bf16 v[160:175], v[196:199], v[132:135], v[160:175]\n"
    "ds_read_b128 v[196:199], v245 offset:50176\n"
    "ds_read_b128 a[180:183], v236 offset:13312\n"
    "ds_write_b64 v235, v[226:227] offset:18432\n"
    "v_exp_f32 v183, v183\n"
    "v_perm_b32 v228, v115, v113, s54\n"
    "v_perm_b32 v229, v119, v117, s54\n"
    "v_lshlrev_b32 v224, 16, v216\n"
    "v_add_f32 v240, v240, v182\n"
    "s_waitcnt lgkmcnt(8)\n"
    "v_mfma_f32_32x32x16_bf16 v[160:175], v[200:203], v[136:139], v[160:175]\n"
    "ds_read_b128 v[200:203], v244 offset:50688\n"
    "ds_read_b128 a[184:187], v236 offset:14336\n"
    "ds_write_b64 v235, v[228:229] offset:19456\n"
    "v_exp_f32 v184, v184\n"
    "v_cvt_pk_bf16_f32 v211, v182, v183\n"
    "v_max3_f32 v234, |v112|, |v113|, v234\n"
    "v_max3_f32 v234, |v114|, |v115|, v234\n"
    "v_max3_f32 v234, |v116|, |v117|, v234\n"
    "v_add_f32 v241, v241, v183\n"
    "v_mfma_f32_32x32x16_bf16 v[160:175], v[204:207], v[140:143], v[160:175]\n"
    "ds_read_b128 v[204:207], v245 offset:50688\n"
    "ds_read_b128 a[188:191], v236 offset:15360\n"
    "v_exp_f32 v185, v185\n"
    "v_max3_f32 v234, |v118|, |v119|, v234\n"
    "v_lshlrev_b32 v230, 16, v112\n"
    "v_lshlrev_b32 v231, 16, v113\n"
    "v_max3_f32 v234, |v230|, |v231|, v234\n"
    "v_lshlrev_b32 v230, 16, v116\n"
    "v_add_f32 v240, v240, v184\n"
    "s_waitcnt lgkmcnt(7)\n"
    "v_mfma_f32_32x32x16_bf16 v[160:175], v[192:195], v[144:147], v[160:175]\n"
    "v_exp_f32 v186, v186\n"
    "v_lshlrev_b32 v231, 16, v117\n"
    "v_max3_f32 v234, |v230|, |v231|, v234\n"
    "v_and_b32 v225, 0xffff0000, v216\n"
    "v_mul_f32 v224, s55, v224\n"
    "v_mul_f32 v225, s55, v225\n"
    "v_cvt_pk_bf16_f32 v216, v224, v225\n"
    "v_lshlrev_b32 v224, 16, v217\n"
    "v_and_b32 v225, 0xffff0000, v217\n"
    "v_add_f32 v241, v241, v185\n"
    "v_mfma_f32_32x32x16_bf16 v[160:175], v[196:199], v[148:151], v[160:175]\n"
    "v_exp_f32 v187, v187\n"
    "v_mul_f32 v224, s55, v224\n"
    "v_mul_f32 v225, s55, v225\n"
    "v_cvt_pk_bf16_f32 v217, v224, v225\n"
    "v_lshlrev_b32 v224, 16, v218\n"
    "v_and_b32 v225, 0xffff0000, v218\n"
    "v_mul_f32 v224, s55, v224\n"
    "v_mul_f32 v225, s55, v225\n"
    "v_add_f32 v240, v240, v186\n"
    "s_waitcnt lgkmcnt(1)\n"
    "v_mfma_f32_32x32x16_bf16 v[160:175], v[200:203], v[152:155], v[160:175]\n"
    "v_exp_f32 v188, v188\n"
    "v_cvt_pk_bf16_f32 v218, v224, v225\n"
    "v_lshlrev_b32 v224, 16, v219\n"
    "v_and_b32 v225, 0xffff0000, v219\n"
    "v_mul_f32 v224, s55, v224\n"
    "v_mul_f32 v225, s55, v225\n"
    "v_cvt_pk_bf16_f32 v219, v224, v225\n"
    "ds_write_b128 v233, v[216:219] offset:57344\n"
    "v_add_f32 v241, v241, v187\n"
    "v_mfma_f32_32x32x16_bf16 v[160:175], v[204:207], v[156:159], v[160:175]\n"
    "v_exp_f32 v189, v189\n"
    "v_lshlrev_b32 v224, 16, v220\n"
    "v_and_b32 v225, 0xffff0000, v220\n"
    "v_mul_f32 v224, s55, v224\n"
    "v_mul_f32 v225, s55, v225\n"
    "v_cvt_pk_bf16_f32 v220, v224, v225\n"
    "v_lshlrev_b32 v224, 16, v221\n"
    "v_and_b32 v225, 0xffff0000, v221\n"
    "v_mul_f32 v224, s55, v224\n"
    "v_add_f32 v240, v240, v188\n"
    "v_mfma_f32_32x32x16_bf16 v[0:15], v[208:211], a[160:163], v[0:15]\n"
    "v_exp_f32 v190, v190\n"
    "v_mul_f32 v225, s55, v225\n"
    "v_cvt_pk_bf16_f32 v221, v224, v225\n"
    "v_lshlrev_b32 v224, 16, v222\n"
    "v_and_b32 v225, 0xffff0000, v222\n"
    "v_mul_f32 v224, s55, v224\n"
    "v_mul_f32 v225, s55, v225\n"
    "v_cvt_pk_bf16_f32 v222, v224, v225\n"
    "v_lshlrev_b32 v224, 16, v223\n"
    "v_add_f32 v241, v241, v189\n"
    "v_mfma_f32_32x32x16_bf16 v[16:31], v[208:211], a[164:167], v[16:31]\n"
    "v_exp_f32 v191, v191\n"
    "v_and_b32 v225, 0xffff0000, v223\n"
    "v_mul_f32 v224, s55, v224\n"
    "v_mul_f32 v225, s55, v225\n"
    "v_cvt_pk_bf16_f32 v223, v224, v225\n"
    "ds_write_b128 v233, v[220:223] offset:58368\n"
    "v_cvt_pk_bf16_f32 v212, v184, v185\n"
    "v_cvt_pk_bf16_f32 v213, v186, v187\n"
    "v_add_f32 v240, v240, v190\n"
    "v_mfma_f32_32x32x16_bf16 v[32:47], v[208:211], a[168:171], v[32:47]\n"
    "buffer_load_dwordx4 v248, s[20:23], s18 offen offset:0 nt lds\n"
    "buffer_load_dwordx4 v249, s[20:23], s18 offen offset:2048 nt lds\n"
    "v_exp_f32 v160, v160\n"
    "v_cvt_pk_bf16_f32 v215, v190, v191\n"
    "v_cvt_pk_bf16_f32 v214, v188, v189\n"
    "v_add_f32 v241, v241, v191\n"
    "v_mfma_f32_32x32x16_bf16 v[48:63], v[208:211], a[172:175], v[48:63]\n"
    "v_exp_f32 v161, v161\n"
    "s_add_u32 m0, s40, 896\n"
    "s_nop 0\n"
    "buffer_load_dwordx4 v248, s[20:23], s18 offen offset:128 nt lds\n"
    "buffer_load_dwordx4 v249, s[20:23], s18 offen offset:2176 nt lds\n"
    "s_add_u32 m0, s40, 4096\n"
    "s_nop 0\n"
    "buffer_load_dwordx4 v248, s[20:23], s19 offen offset:0 nt lds\n"
    "buffer_load_dwordx4 v249, s[20:23], s19 offen offset:2048 nt lds\n"
    "s_add_u32 m0, s40, 4992\n"
    "s_nop 0\n"
    "buffer_load_dwordx4 v248, s[20:23], s19 offen offset:128 nt lds\n"
    "buffer_load_dwordx4 v249, s[20:23], s19 offen offset:2176 nt lds\n"
    "s_waitcnt lgkmcnt(0)\n"
    "s_barrier\n"
    "v_mfma_f32_32x32x16_bf16 v[0:15], v[212:215], a[176:179], v[0:15]\n"
    "ds_read_b128 v[192:195], v244 offset:57344\n"
    "ds_read_b128 a[192:195], v236 offset:16384\n"
    "v_exp_f32 v162, v162\n"
    "v_cvt_pk_bf16_f32 v208, v160, v161\n"
    "v_add_f32 v240, v240, v160\n"
    "v_add_f32 v241, v241, v161\n"
    "v_mfma_f32_32x32x16_bf16 v[16:31], v[212:215], a[180:183], v[16:31]\n"
    "ds_read_b128 v[196:199], v245 offset:57344\n"
    "ds_read_b128 a[196:199], v236 offset:17408\n"
    "v_exp_f32 v163, v163\n"
    "v_add_f32 v240, v240, v162\n"
    "v_mfma_f32_32x32x16_bf16 v[32:47], v[212:215], a[184:187], v[32:47]\n"
    "ds_read_b128 v[200:203], v244 offset:57856\n"
    "ds_read_b128 a[200:203], v236 offset:18432\n"
    "s_waitcnt vmcnt(16)\n"
    "v_exp_f32 v164, v164\n"
    "v_cvt_pk_bf16_f32 v209, v162, v163\n"
    "v_perm_b32 v226, v122, v120, s53\n"
    "v_perm_b32 v227, v126, v124, s53\n"
    "v_max3_f32 v234, |v226|, |v227|, v234\n"
    "v_add_f32 v241, v241, v163\n"
    "v_mfma_f32_32x32x16_bf16 v[48:63], v[212:215], a[188:191], v[48:63]\n"
    "ds_read_b128 v[204:207], v245 offset:57856\n"
    "ds_read_b128 a[204:207], v236 offset:19456\n"
    "ds_write_b64 v235, v[226:227] offset:24576\n"
    "v_exp_f32 v165, v165\n"
    "v_perm_b32 v228, v122, v120, s54\n"
    "v_perm_b32 v229, v126, v124, s54\n"
    "v_add_f32 v240, v240, v164\n"
    "s_waitcnt lgkmcnt(6)\n"
    "v_mfma_f32_32x32x16_bf16 v[176:191], v[192:195], v[128:131], 0\n"
    "ds_read_b128 v[192:195], v244 offset:58368\n"
    "ds_read_b128 a[208:211], v236 offset:20480\n"
    "ds_write_b64 v235, v[228:229] offset:25600\n"
    "s_waitcnt vmcnt(8)\n"
    "v_exp_f32 v166, v166\n"
    "v_cvt_pk_bf16_f32 v210, v164, v165\n"
    "v_perm_b32 v226, v123, v121, s53\n"
    "v_perm_b32 v227, v127, v125, s53\n"
    "v_mfma_f32_32x32x16_bf16 v[176:191], v[196:199], v[132:135], v[176:191]\n"
    "ds_read_b128 v[196:199], v245 offset:58368\n"
    "ds_read_b128 a[212:215], v236 offset:21504\n"
    "v_exp_f32 v167, v167\n"
    "v_max3_f32 v234, |v226|, |v227|, v234\n"
    "ds_write_b64 v235, v[226:227] offset:26624\n"
    "v_perm_b32 v228, v123, v121, s54\n"
    "v_perm_b32 v229, v127, v125, s54\n"
    "s_waitcnt lgkmcnt(8)\n"
    "v_mfma_f32_32x32x16_bf16 v[176:191], v[200:203], v[136:139], v[176:191]\n"
    "ds_read_b128 v[200:203], v244 offset:58880\n"
    "ds_read_b128 a[216:219], v236 offset:22528\n"
    "ds_write_b64 v235, v[228:229] offset:27648\n"
    "v_exp_f32 v168, v168\n"
    "v_cvt_pk_bf16_f32 v211, v166, v167\n"
    "v_max3_f32 v234, |v120|, |v121|, v234\n"
    "v_max3_f32 v234, |v122|, |v123|, v234\n"
    "v_max3_f32 v234, |v124|, |v125|, v234\n"
    "v_mfma_f32_32x32x16_bf16 v[176:191], v[204:207], v[140:143], v[176:191]\n"
    "ds_read_b128 v[204:207], v245 offset:58880\n"
    "ds_read_b128 a[220:223], v236 offset:23552\n"
    "v_exp_f32 v169, v169\n"
    "v_max3_f32 v234, |v126|, |v127|, v234\n"
    "v_lshlrev_b32 v230, 16, v120\n"
    "v_lshlrev_b32 v231, 16, v121\n"
    "v_max3_f32 v234, |v230|, |v231|, v234\n"
    "v_lshlrev_b32 v230, 16, v124\n"
    "s_waitcnt lgkmcnt(7)\n"
    "v_mfma_f32_32x32x16_bf16 v[176:191], v[192:195], v[144:147], v[176:191]\n"
    "v_exp_f32 v170, v170\n"
    "v_lshlrev_b32 v231, 16, v125\n"
    "v_max3_f32 v234, |v230|, |v231|, v234\n"
    "v_mbcnt_lo_u32_b32 v230, -1, 0\n"
    "v_mbcnt_hi_u32_b32 v230, -1, v230\n"
    "v_lshlrev_b32 v230, 2, v230\n"
    "s_lshl_b32 s58, s36, 8\n"
    "s_add_u32 s58, s58, 131072\n"
    "v_add_u32 v230, s58, v230\n"
    "v_mfma_f32_32x32x16_bf16 v[176:191], v[196:199], v[148:151], v[176:191]\n"
    "ds_write_b32 v230, v234\n"
    "v_exp_f32 v171, v171\n"
    "v_cvt_pk_bf16_f32 v212, v168, v169\n"
    "v_add_f32 v241, v241, v165\n"
    "v_add_f32 v240, v240, v166\n"
    "v_add_f32 v241, v241, v167\n"
    "v_add_f32 v240, v240, v168\n"
    "s_waitcnt lgkmcnt(2)\n"
    "v_mfma_f32_32x32x16_bf16 v[176:191], v[200:203], v[152:155], v[176:191]\n"
    "ds_read_b128 v[128:131], v246 offset:32768\n"
    "ds_read_b128 v[132:135], v247 offset:32768\n"
    "ds_read_b128 v[136:139], v246 offset:33280\n"
    "ds_read_b128 v[140:143], v247 offset:33280\n"
    "ds_read_b128 v[144:147], v246 offset:33792\n"
    "v_exp_f32 v172, v172\n"
    "v_cvt_pk_bf16_f32 v213, v170, v171\n"
    "v_add_f32 v241, v241, v169\n"
    "v_add_f32 v240, v240, v170\n"
    "v_mfma_f32_32x32x16_bf16 v[176:191], v[204:207], v[156:159], v[176:191]\n"
    "ds_read_b128 v[148:151], v247 offset:33792\n"
    "ds_read_b128 v[152:155], v246 offset:34304\n"
    "v_exp_f32 v173, v173\n"
    "v_add_f32 v241, v241, v171\n"
    "v_add_f32 v240, v240, v172\n"
    "v_mfma_f32_32x32x16_bf16 v[0:15], v[208:211], a[192:195], v[0:15]\n"
    "ds_read_b128 v[156:159], v247 offset:34304\n"
    "v_exp_f32 v174, v174\n"
    "v_cvt_pk_bf16_f32 v214, v172, v173\n"
    "v_add_f32 v241, v241, v173\n"
    "v_mfma_f32_32x32x16_bf16 v[16:31], v[208:211], a[196:199], v[16:31]\n"
    "v_exp_f32 v175, v175\n"
    "v_add_f32 v240, v240, v174\n"
    "v_mfma_f32_32x32x16_bf16 v[32:47], v[208:211], a[200:203], v[32:47]\n"
    "v_exp_f32 v176, v176\n"
    "v_cvt_pk_bf16_f32 v215, v174, v175\n"
    "v_add_f32 v241, v241, v175\n"
    "v_mfma_f32_32x32x16_bf16 v[48:63], v[208:211], a[204:207], v[48:63]\n"
    "v_exp_f32 v177, v177\n"
    "s_waitcnt lgkmcnt(0)\n"
    "s_barrier\n"
    "v_mfma_f32_32x32x16_bf16 v[0:15], v[212:215], a[208:211], v[0:15]\n"
    "ds_read_b128 v[192:195], v244 offset:0\n"
    "ds_read_b128 a[224:227], v236 offset:24576\n"
    "v_exp_f32 v178, v178\n"
    "v_cvt_pk_bf16_f32 v208, v176, v177\n"
    "v_add_f32 v240, v240, v176\n"
    "v_add_f32 v241, v241, v177\n"
    "v_mbcnt_lo_u32_b32 v230, -1, 0\n"
    "v_mbcnt_hi_u32_b32 v230, -1, v230\n"
    "v_lshlrev_b32 v230, 2, v230\n"
    "v_mfma_f32_32x32x16_bf16 v[16:31], v[212:215], a[212:215], v[16:31]\n"
    "ds_read_b128 v[196:199], v245 offset:0\n"
    "ds_read_b128 a[228:231], v236 offset:25600\n"
    "v_exp_f32 v179, v179\n"
    "v_add_u32 v230, 131072, v230\n"
    "ds_read_b32 v216, v230 offset:0\n"
    "ds_read_b32 v217, v230 offset:256\n"
    "ds_read_b32 v218, v230 offset:512\n"
    "ds_read_b32 v219, v230 offset:768\n"
    "s_waitcnt lgkmcnt(2)\n"
    "v_max_f32 v216, v216, v217\n"
    "v_mfma_f32_32x32x16_bf16 v[32:47], v[212:215], a[216:219], v[32:47]\n"
    "ds_read_b128 v[200:203], v244 offset:512\n"
    "ds_read_b128 a[232:235], v236 offset:26624\n"
    "v_exp_f32 v180, v180\n"
    "v_cvt_pk_bf16_f32 v209, v178, v179\n"
    "s_waitcnt lgkmcnt(2)\n"
    "v_max3_f32 v216, v216, v218, v219\n"
    "s_mov_b32 s58, 0x5d800000\n"
    "s_mov_b32 s59, 0x2b800000\n"
    "v_cmp_lt_f32 vcc, s58, v216\n"
    "v_cmp_le_f32 s[18:19], s59, v216\n"
    "v_mfma_f32_32x32x16_bf16 v[48:63], v[212:215], a[220:223], v[48:63]\n"
    "ds_read_b128 v[204:207], v245 offset:512\n"
    "ds_read_b128 a[236:239], v236 offset:27648\n"
    "v_exp_f32 v181, v181\n"
    "v_mov_b32 v253, 1.0\n"
    "s_nop 0\n"
    "v_mov_b32 v224, 0x7f800000\n"
    "s_cmp_eq_u64 s[18:19], 0\n"
    "s_cselect_b64 s[18:19], -1, 0\n"
    "v_mfma_f32_32x32x16_bf16 v[160:175], v[192:195], v[128:131], 0\n"
    "ds_read_b128 v[192:195], v244 offset:1024\n"
    "ds_read_b128 a[240:243], v236 offset:28672\n"
    "v_exp_f32 v182, v182\n"
    "v_cvt_pk_bf16_f32 v210, v180, v181\n"
    "s_or_b64 vcc, vcc, s[18:19]\n"
    "s_cmp_lg_u64 vcc, 0\n"
    "s_cselect_b64 vcc, -1, 0\n"
    "s_nop 0\n"
    "v_cndmask_b32 v253, v253, v224, vcc\n"
    "v_mfma_f32_32x32x16_bf16 v[160:175], v[196:199], v[132:135], v[160:175]\n"
    "ds_read_b128 v[196:199], v245 offset:1024\n"
    "ds_read_b128 a[244:247], v236 offset:29696\n"
    "v_exp_f32 v183, v183\n"
    "v_add_f32 v240, v240, v178\n"
    "v_add_f32 v241, v241, v179\n"
    "v_add_f32 v240, v240, v180\n"
    "v_add_f32 v241, v241, v181\n"
    "v_add_f32 v240, v240, v182\n"
    "s_waitcnt lgkmcnt(5)\n"
    "v_mfma_f32_32x32x16_bf16 v[160:175], v[200:203], v[136:139], v[160:175]\n"
    "ds_read_b128 v[200:203], v244 offset:1536\n"
    "ds_read_b128 a[248:251], v236 offset:30720\n"
    "v_exp_f32 v184, v184\n"
    "v_cvt_pk_bf16_f32 v211, v182, v183\n"
    "v_add_f32 v241, v241, v183\n"
    "v_mfma_f32_32x32x16_bf16 v[160:175], v[204:207], v[140:143], v[160:175]\n"
    "ds_read_b128 v[204:207], v245 offset:1536\n"
    "ds_read_b128 a[252:255], v236 offset:31744\n"
    "v_exp_f32 v185, v185\n"
    "v_add_f32 v240, v240, v184\n"
    "s_waitcnt lgkmcnt(5)\n"
    "v_mfma_f32_32x32x16_bf16 v[160:175], v[192:195], v[144:147], v[160:175]\n"
    "v_exp_f32 v186, v186\n"
    "v_cvt_pk_bf16_f32 v212, v184, v185\n"
    "v_add_f32 v241, v241, v185\n"
    "v_mfma_f32_32x32x16_bf16 v[160:175], v[196:199], v[148:151], v[160:175]\n"
    "v_exp_f32 v187, v187\n"
    "v_add_f32 v240, v240, v186\n"
    "s_waitcnt lgkmcnt(1)\n"
    "v_mfma_f32_32x32x16_bf16 v[160:175], v[200:203], v[152:155], v[160:175]\n"
    "v_exp_f32 v188, v188\n"
    "v_cvt_pk_bf16_f32 v213, v186, v187\n"
    "v_add_f32 v241, v241, v187\n"
    "v_mfma_f32_32x32x16_bf16 v[160:175], v[204:207], v[156:159], v[160:175]\n"
    "v_exp_f32 v189, v189\n"
    "v_add_f32 v240, v240, v188\n"
    "v_mfma_f32_32x32x16_bf16 v[0:15], v[208:211], a[224:227], v[0:15]\n"
    "v_exp_f32 v190, v190\n"
    "v_cvt_pk_bf16_f32 v214, v188, v189\n"
    "v_add_f32 v241, v241, v189\n"
    "v_mfma_f32_32x32x16_bf16 v[16:31], v[208:211], a[228:231], v[16:31]\n"
    "v_exp_f32 v191, v191\n"
    "v_add_f32 v240, v240, v190\n"
    "v_mfma_f32_32x32x16_bf16 v[32:47], v[208:211], a[232:235], v[32:47]\n"
    "s_nop 1\n"
    "v_exp_f32 v160, v160\n"
    "v_cvt_pk_bf16_f32 v215, v190, v191\n"
    "v_add_f32 v241, v241, v191\n"
    "v_mfma_f32_32x32x16_bf16 v[48:63], v[208:211], a[236:239], v[48:63]\n"
    "v_exp_f32 v161, v161\n"
    "s_add_u32 s24, s48, 4096\n"
    "s_addc_u32 s25, s49, 0\n"
    "s_mov_b32 s26, 0x3f000\n"
    "s_waitcnt lgkmcnt(0)\n"
    "Lloop_%=:\n"
    "v_mfma_f32_32x32x16_bf16 v[0:15], v[212:215], a[240:243], v[0:15]\n"
    "ds_read_b128 v[192:195], v244 offset:8192\n"
    "v_exp_f32 v162, v162\n"
    "v_add_f32 v240, v240, v241\n"
    "v_mov_b32 v255, v240\n"
    "v_add_f32 v242, v160, v161\n"
    "v_mfma_f32_32x32x16_bf16 v[16:31], v[212:215], a[244:247], v[16:31]\n"
    "ds_read_b128 v[196:199], v245 offset:8192\n"
    "v_exp_f32 v163, v163\n"
    "v_permlane32_swap_b32 v240, v255\n"
    "v_add_f32 v240, v240, v255\n"
    "s_add_u32 s45, s45, 32768\n"
    "v_mfma_f32_32x32x16_bf16 v[32:47], v[212:215], a[248:251], v[32:47]\n"
    "ds_read_b128 v[200:203], v244 offset:8704\n"
    "v_exp_f32 v164, v164\n"
    "v_add_f32 v243, v162, v163\n"
    "v_rcp_f32 v255, v240\n"
    "v_mfma_f32_32x32x16_bf16 v[48:63], v[212:215], a[252:255], v[48:63]\n"
    "ds_read_b128 v[204:207], v245 offset:8704\n"
    "ds_write_b32 v251, v255\n"
    "v_exp_f32 v165, v165\n"
    "s_waitcnt lgkmcnt(3)\n"
    "v_mfma_f32_32x32x16_bf16 v[176:191], v[192:195], v[128:131], 0\n"
    "ds_read_b128 v[192:195], v244 offset:9216\n"
    "ds_read_b128 v[216:219], v252 offset:0\n"
    "v_exp_f32 v166, v166\n"
    "s_cmp_ge_u32 s52, 3\n"
    "s_cselect_b32 s22, 0, 0x40000\n"
    "v_mfma_f32_32x32x16_bf16 v[176:191], v[196:199], v[132:135], v[176:191]\n"
    "ds_read_b128 v[196:199], v245 offset:9216\n"
    "ds_read_b128 v[220:223], v252 offset:32\n"
    "v_exp_f32 v167, v167\n"
    "v_cvt_pk_bf16_f32 v208, v160, v161\n"
    "s_waitcnt lgkmcnt(5)\n"
    "v_mfma_f32_32x32x16_bf16 v[176:191], v[200:203], v[136:139], v[176:191]\n"
    "ds_read_b128 v[200:203], v244 offset:9728\n"
    "ds_read_b128 v[224:227], v252 offset:64\n"
    "v_exp_f32 v168, v168\n"
    "v_cvt_pk_bf16_f32 v209, v162, v163\n"
    "v_cvt_pk_bf16_f32 v210, v164, v165\n"
    "v_mfma_f32_32x32x16_bf16 v[176:191], v[204:207], v[140:143], v[176:191]\n"
    "ds_read_b128 v[204:207], v245 offset:9728\n"
    "ds_read_b128 v[228:231], v252 offset:96\n"
    "v_exp_f32 v169, v169\n"
    "v_cvt_pk_bf16_f32 v211, v166, v167\n"
    "s_waitcnt lgkmcnt(5)\n"
    "v_mfma_f32_32x32x16_bf16 v[176:191], v[192:195], v[144:147], v[176:191]\n"
    "v_exp_f32 v170, v170\n"
    "v_cvt_pk_bf16_f32 v212, v168, v169\n"
    "v_max3_f32 v253, v253, v240, v255\n"
    "v_add_f32 v242, v242, v164\n"
    "v_add_f32 v243, v243, v165\n"
    "v_mfma_f32_32x32x16_bf16 v[176:191], v[196:199], v[148:151], v[176:191]\n"
    "v_exp_f32 v171, v171\n"
    "v_add_f32 v242, v242, v166\n"
    "v_add_f32 v243, v243, v167\n"
    "v_add_f32 v242, v242, v168\n"
    "s_waitcnt lgkmcnt(1)\n"
    "v_mfma_f32_32x32x16_bf16 v[176:191], v[200:203], v[152:155], v[176:191]\n"
    "v_exp_f32 v172, v172\n"
    "v_cvt_pk_bf16_f32 v213, v170, v171\n"
    "v_add_f32 v243, v243, v169\n"
    "v_add_f32 v242, v242, v170\n"
    "v_add_f32 v243, v243, v171\n"
    "v_mfma_f32_32x32x16_bf16 v[176:191], v[204:207], v[156:159], v[176:191]\n"
    "v_exp_f32 v173, v173\n"
    "v_add_f32 v242, v242, v172\n"
    "v_mul_f32 v0, v0, v216\n"
    "v_mul_f32 v16, v16, v216\n"
    "v_mul_f32 v32, v32, v216\n"
    "v_mfma_f32_32x32x16_bf16 v[64:79], v[208:211], a[0:3], 0\n"
    "v_exp_f32 v174, v174\n"
    "v_cvt_pk_bf16_f32 v214, v172, v173\n"
    "v_add_f32 v243, v243, v173\n"
    "v_mul_f32 v48, v48, v216\n"
    "v_cvt_pk_bf16_f32 v232, v0, v16\n"
    "v_mfma_f32_32x32x16_bf16 v[80:95], v[208:211], a[4:7], 0\n"
    "v_exp_f32 v175, v175\n"
    "v_add_f32 v242, v242, v174\n"
    "v_cvt_pk_bf16_f32 v233, v32, v48\n"
    "s_add_u32 m0, s40, 32768\n"
    "v_mfma_f32_32x32x16_bf16 v[96:111], v[208:211], a[8:11], 0\n"
    "v_exp_f32 v176, v176\n"
    "v_cvt_pk_bf16_f32 v215, v174, v175\n"
    "v_add_f32 v243, v243, v175\n"
    "v_mfma_f32_32x32x16_bf16 v[112:127], v[208:211], a[12:15], 0\n"
    "buffer_store_dwordx2 v[232:233], v250, s[48:51], s45 offen offset:0 sc0 sc1\n"
    "v_exp_f32 v177, v177\n"
    "v_mfma_f32_32x32x16_bf16 v[64:79], v[212:215], a[16:19], v[64:79]\n"
    "ds_read_b128 v[192:195], v244 offset:16384\n"
    "v_exp_f32 v178, v178\n"
    "v_cvt_pk_bf16_f32 v208, v176, v177\n"
    "v_add_f32 v242, v242, v176\n"
    "v_add_f32 v243, v243, v177\n"
    "v_mfma_f32_32x32x16_bf16 v[80:95], v[212:215], a[20:23], v[80:95]\n"
    "ds_read_b128 v[196:199], v245 offset:16384\n"
    "v_exp_f32 v179, v179\n"
    "v_mul_f32 v1, v1, v217\n"
    "v_mul_f32 v17, v17, v217\n"
    "v_mul_f32 v33, v33, v217\n"
    "v_mfma_f32_32x32x16_bf16 v[96:111], v[212:215], a[24:27], v[96:111]\n"
    "ds_read_b128 v[200:203], v244 offset:16896\n"
    "v_exp_f32 v180, v180\n"
    "v_cvt_pk_bf16_f32 v209, v178, v179\n"
    "v_mul_f32 v49, v49, v217\n"
    "v_cvt_pk_bf16_f32 v234, v1, v17\n"
    "v_mfma_f32_32x32x16_bf16 v[112:127], v[212:215], a[28:31], v[112:127]\n"
    "ds_read_b128 v[204:207], v245 offset:16896\n"
    "v_exp_f32 v181, v181\n"
    "v_cvt_pk_bf16_f32 v235, v33, v49\n"
    "v_add_f32 v242, v242, v178\n"
    "s_waitcnt lgkmcnt(2)\n"
    "v_mfma_f32_32x32x16_bf16 v[160:175], v[192:195], v[128:131], 0\n"
    "ds_read_b128 v[192:195], v244 offset:17408\n"
    "v_exp_f32 v182, v182\n"
    "v_cvt_pk_bf16_f32 v210, v180, v181\n"
    "v_add_f32 v243, v243, v179\n"
    "v_add_f32 v242, v242, v180\n"
    "v_mfma_f32_32x32x16_bf16 v[160:175], v[196:199], v[132:135], v[160:175]\n"
    "ds_read_b128 v[196:199], v245 offset:17408\n"
    "v_exp_f32 v183, v183\n"
    "v_add_f32 v243, v243, v181\n"
    "v_add_f32 v242, v242, v182\n"
    "s_waitcnt lgkmcnt(2)\n"
    "v_mfma_f32_32x32x16_bf16 v[160:175], v[200:203], v[136:139], v[160:175]\n"
    "ds_read_b128 v[200:203], v244 offset:17920\n"
    "v_exp_f32 v184, v184\n"
    "v_cvt_pk_bf16_f32 v211, v182, v183\n"
    "v_add_f32 v243, v243, v183\n"
    "v_mfma_f32_32x32x16_bf16 v[160:175], v[204:207], v[140:143], v[160:175]\n"
    "ds_read_b128 v[204:207], v245 offset:17920\n"
    "v_exp_f32 v185, v185\n"
    "v_add_f32 v242, v242, v184\n"
    "s_waitcnt lgkmcnt(2)\n"
    "v_mfma_f32_32x32x16_bf16 v[160:175], v[192:195], v[144:147], v[160:175]\n"
    "v_exp_f32 v186, v186\n"
    "v_cvt_pk_bf16_f32 v212, v184, v185\n"
    "v_add_f32 v243, v243, v185\n"
    "v_mul_f32 v2, v2, v218\n"
    "v_mul_f32 v18, v18, v218\n"
    "v_mfma_f32_32x32x16_bf16 v[160:175], v[196:199], v[148:151], v[160:175]\n"
    "v_exp_f32 v187, v187\n"
    "v_add_f32 v242, v242, v186\n"
    "v_mul_f32 v34, v34, v218\n"
    "v_mul_f32 v50, v50, v218\n"
    "s_waitcnt lgkmcnt(0)\n"
    "v_mfma_f32_32x32x16_bf16 v[160:175], v[200:203], v[152:155], v[160:175]\n"
    "v_exp_f32 v188, v188\n"
    "v_cvt_pk_bf16_f32 v213, v186, v187\n"
    "v_add_f32 v243, v243, v187\n"
    "v_cvt_pk_bf16_f32 v236, v2, v18\n"
    "v_cvt_pk_bf16_f32 v237, v34, v50\n"
    "v_mfma_f32_32x32x16_bf16 v[160:175], v[204:207], v[156:159], v[160:175]\n"
    "buffer_load_dwordx4 v248, s[20:23], s41 offen offset:0 nt lds\n"
    "v_exp_f32 v189, v189\n"
    "v_add_f32 v242, v242, v188\n"
    "v_mfma_f32_32x32x16_bf16 v[64:79], v[208:211], a[32:35], v[64:79]\n"
    "v_exp_f32 v190, v190\n"
    "v_cvt_pk_bf16_f32 v214, v188, v189\n"
    "v_add_f32 v243, v243, v189\n"
    "v_mfma_f32_32x32x16_bf16 v[80:95], v[208:211], a[36:39], v[80:95]\n"
    "buffer_load_dwordx4 v249, s[20:23], s41 offen offset:2048 nt lds\n"
    "v_exp_f32 v191, v191\n"
    "v_add_f32 v242, v242, v190\n"
    "v_mfma_f32_32x32x16_bf16 v[96:111], v[208:211], a[40:43], v[96:111]\n"
    "v_exp_f32 v160, v160\n"
    "v_cvt_pk_bf16_f32 v215, v190, v191\n"
    "v_add_f32 v243, v243, v191\n"
    "s_add_u32 m0, s40, 33664\n"
    "v_mfma_f32_32x32x16_bf16 v[112:127], v[208:211], a[44:47], v[112:127]\n"
    "buffer_store_dwordx2 v[234:235], v250, s[48:51], s45 offen offset:256 sc0 sc1\n"
    "v_exp_f32 v161, v161\n"
    "v_mfma_f32_32x32x16_bf16 v[64:79], v[212:215], a[48:51], v[64:79]\n"
    "ds_read_b128 v[192:195], v244 offset:24576\n"
    "buffer_store_dwordx2 v[236:237], v250, s[48:51], s45 offen offset:512 sc0 sc1\n"
    "v_exp_f32 v162, v162\n"
    "v_mfma_f32_32x32x16_bf16 v[80:95], v[212:215], a[52:55], v[80:95]\n"
    "ds_read_b128 v[196:199], v245 offset:24576\n"
    "v_exp_f32 v163, v163\n"
    "v_cvt_pk_bf16_f32 v208, v160, v161\n"
    "v_mul_f32 v3, v3, v219\n"
    "v_mul_f32 v19, v19, v219\n"
    "v_mfma_f32_32x32x16_bf16 v[96:111], v[212:215], a[56:59], v[96:111]\n"
    "ds_read_b128 v[200:203], v244 offset:25088\n"
    "v_exp_f32 v164, v164\n"
    "v_cvt_pk_bf16_f32 v209, v162, v163\n"
    "v_mul_f32 v35, v35, v219\n"
    "v_mul_f32 v51, v51, v219\n"
    "v_mfma_f32_32x32x16_bf16 v[112:127], v[212:215], a[60:63], v[112:127]\n"
    "ds_read_b128 v[204:207], v245 offset:25088\n"
    "v_exp_f32 v165, v165\n"
    "v_cvt_pk_bf16_f32 v238, v3, v19\n"
    "v_cvt_pk_bf16_f32 v239, v35, v51\n"
    "s_waitcnt lgkmcnt(2)\n"
    "v_mfma_f32_32x32x16_bf16 v[176:191], v[192:195], v[128:131], 0\n"
    "ds_read_b128 v[192:195], v244 offset:25600\n"
    "v_exp_f32 v166, v166\n"
    "v_cvt_pk_bf16_f32 v210, v164, v165\n"
    "v_add_f32 v242, v242, v160\n"
    "v_add_f32 v243, v243, v161\n"
    "v_mfma_f32_32x32x16_bf16 v[176:191], v[196:199], v[132:135], v[176:191]\n"
    "ds_read_b128 v[196:199], v245 offset:25600\n"
    "v_exp_f32 v167, v167\n"
    "v_add_f32 v242, v242, v162\n"
    "v_add_f32 v243, v243, v163\n"
    "s_waitcnt lgkmcnt(2)\n"
    "v_mfma_f32_32x32x16_bf16 v[176:191], v[200:203], v[136:139], v[176:191]\n"
    "ds_read_b128 v[200:203], v244 offset:26112\n"
    "v_exp_f32 v168, v168\n"
    "v_cvt_pk_bf16_f32 v211, v166, v167\n"
    "v_add_f32 v242, v242, v164\n"
    "v_add_f32 v243, v243, v165\n"
    "v_mfma_f32_32x32x16_bf16 v[176:191], v[204:207], v[140:143], v[176:191]\n"
    "ds_read_b128 v[204:207], v245 offset:26112\n"
    "v_exp_f32 v169, v169\n"
    "v_add_f32 v242, v242, v166\n"
    "v_add_f32 v243, v243, v167\n"
    "s_waitcnt lgkmcnt(2)\n"
    "v_mfma_f32_32x32x16_bf16 v[176:191], v[192:195], v[144:147], v[176:191]\n"
    "buffer_store_dwordx2 v[238:239], v250, s[48:51], s45 offen offset:768 sc0 sc1\n"
    "v_exp_f32 v170, v170\n"
    "v_mfma_f32_32x32x16_bf16 v[176:191], v[196:199], v[148:151], v[176:191]\n"
    "v_exp_f32 v171, v171\n"
    "v_cvt_pk_bf16_f32 v212, v168, v169\n"
    "v_add_f32 v242, v242, v168\n"
    "v_add_f32 v243, v243, v169\n"
    "s_waitcnt lgkmcnt(0)\n"
    "v_mfma_f32_32x32x16_bf16 v[176:191], v[200:203], v[152:155], v[176:191]\n"
    "v_exp_f32 v172, v172\n"
    "v_cvt_pk_bf16_f32 v213, v170, v171\n"
    "v_add_f32 v242, v242, v170\n"
    "v_add_f32 v243, v243, v171\n"
    "v_mul_f32 v4, v4, v220\n"
    "v_mfma_f32_32x32x16_bf16 v[176:191], v[204:207], v[156:159], v[176:191]\n"
    "v_exp_f32 v173, v173\n"
    "v_add_f32 v242, v242, v172\n"
    "v_mul_f32 v20, v20, v220\n"
    "v_mul_f32 v36, v36, v220\n"
    "v_mul_f32 v52, v52, v220\n"
    "v_mfma_f32_32x32x16_bf16 v[64:79], v[208:211], a[64:67], v[64:79]\n"
    "v_exp_f32 v174, v174\n"
    "v_cvt_pk_bf16_f32 v214, v172, v173\n"
    "v_add_f32 v243, v243, v173\n"
    "v_cvt_pk_bf16_f32 v232, v4, v20\n"
    "v_cvt_pk_bf16_f32 v233, v36, v52\n"
    "v_mfma_f32_32x32x16_bf16 v[80:95], v[208:211], a[68:71], v[80:95]\n"
    "v_exp_f32 v175, v175\n"
    "v_add_f32 v242, v242, v174\n"
    "v_mul_f32 v5, v5, v221\n"
    "v_mul_f32 v21, v21, v221\n"
    "v_mul_f32 v37, v37, v221\n"
    "v_mfma_f32_32x32x16_bf16 v[96:111], v[208:211], a[72:75], v[96:111]\n"
    "v_exp_f32 v176, v176\n"
    "v_cvt_pk_bf16_f32 v215, v174, v175\n"
    "v_add_f32 v243, v243, v175\n"
    "v_mul_f32 v53, v53, v221\n"
    "v_cvt_pk_bf16_f32 v234, v5, v21\n"
    "v_mfma_f32_32x32x16_bf16 v[112:127], v[208:211], a[76:79], v[112:127]\n"
    "buffer_store_dwordx2 v[232:233], v250, s[48:51], s45 offen offset:2048 sc0 sc1\n"
    "v_exp_f32 v177, v177\n"
    "v_mfma_f32_32x32x16_bf16 v[64:79], v[212:215], a[80:83], v[64:79]\n"
    "ds_read_b128 v[192:195], v244 offset:32768\n"
    "v_exp_f32 v178, v178\n"
    "v_cvt_pk_bf16_f32 v235, v37, v53\n"
    "v_cvt_pk_bf16_f32 v208, v176, v177\n"
    "v_add_f32 v242, v242, v176\n"
    "v_mfma_f32_32x32x16_bf16 v[80:95], v[212:215], a[84:87], v[80:95]\n"
    "ds_read_b128 v[196:199], v245 offset:32768\n"
    "v_exp_f32 v179, v179\n"
    "v_add_f32 v243, v243, v177\n"
    "v_add_f32 v242, v242, v178\n"
    "v_mfma_f32_32x32x16_bf16 v[96:111], v[212:215], a[88:91], v[96:111]\n"
    "ds_read_b128 v[200:203], v244 offset:33280\n"
    "v_exp_f32 v180, v180\n"
    "v_cvt_pk_bf16_f32 v209, v178, v179\n"
    "v_add_f32 v243, v243, v179\n"
    "v_mfma_f32_32x32x16_bf16 v[112:127], v[212:215], a[92:95], v[112:127]\n"
    "ds_read_b128 v[204:207], v245 offset:33280\n"
    "v_exp_f32 v181, v181\n"
    "v_add_f32 v242, v242, v180\n"
    "s_waitcnt lgkmcnt(2)\n"
    "v_mfma_f32_32x32x16_bf16 v[160:175], v[192:195], v[128:131], 0\n"
    "ds_read_b128 v[192:195], v244 offset:33792\n"
    "v_exp_f32 v182, v182\n"
    "v_cvt_pk_bf16_f32 v210, v180, v181\n"
    "v_add_f32 v243, v243, v181\n"
    "v_mul_f32 v6, v6, v222\n"
    "v_mfma_f32_32x32x16_bf16 v[160:175], v[196:199], v[132:135], v[160:175]\n"
    "ds_read_b128 v[196:199], v245 offset:33792\n"
    "v_exp_f32 v183, v183\n"
    "v_add_f32 v242, v242, v182\n"
    "v_mul_f32 v22, v22, v222\n"
    "s_waitcnt lgkmcnt(2)\n"
    "v_mfma_f32_32x32x16_bf16 v[160:175], v[200:203], v[136:139], v[160:175]\n"
    "ds_read_b128 v[200:203], v244 offset:34304\n"
    "v_exp_f32 v184, v184\n"
    "v_cvt_pk_bf16_f32 v211, v182, v183\n"
    "v_add_f32 v243, v243, v183\n"
    "v_mul_f32 v38, v38, v222\n"
    "v_mfma_f32_32x32x16_bf16 v[160:175], v[204:207], v[140:143], v[160:175]\n"
    "ds_read_b128 v[204:207], v245 offset:34304\n"
    "v_exp_f32 v185, v185\n"
    "v_add_f32 v242, v242, v184\n"
    "v_mul_f32 v54, v54, v222\n"
    "s_waitcnt lgkmcnt(2)\n"
    "v_mfma_f32_32x32x16_bf16 v[160:175], v[192:195], v[144:147], v[160:175]\n"
    "buffer_store_dwordx2 v[234:235], v250, s[48:51], s45 offen offset:2304 sc0 sc1\n"
    "v_exp_f32 v186, v186\n"
    "v_mfma_f32_32x32x16_bf16 v[160:175], v[196:199], v[148:151], v[160:175]\n"
    "v_exp_f32 v187, v187\n"
    "v_cvt_pk_bf16_f32 v212, v184, v185\n"
    "v_add_f32 v243, v243, v185\n"
    "v_add_f32 v242, v242, v186\n"
    "s_waitcnt lgkmcnt(0)\n"
    "v_mfma_f32_32x32x16_bf16 v[160:175], v[200:203], v[152:155], v[160:175]\n"
    "v_exp_f32 v188, v188\n"
    "v_cvt_pk_bf16_f32 v213, v186, v187\n"
    "v_add_f32 v243, v243, v187\n"
    "v_cvt_pk_bf16_f32 v236, v6, v22\n"
    "v_cvt_pk_bf16_f32 v237, v38, v54\n"
    "v_mfma_f32_32x32x16_bf16 v[160:175], v[204:207], v[156:159], v[160:175]\n"
    "v_exp_f32 v189, v189\n"
    "v_add_f32 v242, v242, v188\n"
    "v_mul_f32 v7, v7, v223\n"
    "v_mul_f32 v23, v23, v223\n"
    "v_mul_f32 v39, v39, v223\n"
    "v_mfma_f32_32x32x16_bf16 v[64:79], v[208:211], a[96:99], v[64:79]\n"
    "v_exp_f32 v190, v190\n"
    "v_cvt_pk_bf16_f32 v214, v188, v189\n"
    "v_add_f32 v243, v243, v189\n"
    "v_mul_f32 v55, v55, v223\n"
    "v_cvt_pk_bf16_f32 v238, v7, v23\n"
    "v_mfma_f32_32x32x16_bf16 v[80:95], v[208:211], a[100:103], v[80:95]\n"
    "v_exp_f32 v191, v191\n"
    "v_add_f32 v242, v242, v190\n"
    "v_cvt_pk_bf16_f32 v239, v39, v55\n"
    "v_mfma_f32_32x32x16_bf16 v[96:111], v[208:211], a[104:107], v[96:111]\n"
    "v_exp_f32 v160, v160\n"
    "v_cvt_pk_bf16_f32 v215, v190, v191\n"
    "v_add_f32 v243, v243, v191\n"
    "v_mfma_f32_32x32x16_bf16 v[112:127], v[208:211], a[108:111], v[112:127]\n"
    "buffer_store_dwordx2 v[236:237], v250, s[48:51], s45 offen offset:2560 sc0 sc1\n"
    "v_exp_f32 v161, v161\n"
    "v_mfma_f32_32x32x16_bf16 v[64:79], v[212:215], a[112:115], v[64:79]\n"
    "ds_read_b128 v[192:195], v244 offset:40960\n"
    "v_exp_f32 v162, v162\n"
    "v_cvt_pk_bf16_f32 v208, v160, v161\n"
    "v_add_f32 v242, v242, v160\n"
    "v_add_f32 v243, v243, v161\n"
    "v_mfma_f32_32x32x16_bf16 v[80:95], v[212:215], a[116:119], v[80:95]\n"
    "ds_read_b128 v[196:199], v245 offset:40960\n"
    "buffer_load_dwordx4 v248, s[20:23], s41 offen offset:128 nt lds\n"
    "v_exp_f32 v163, v163\n"
    "v_mfma_f32_32x32x16_bf16 v[96:111], v[212:215], a[120:123], v[96:111]\n"
    "ds_read_b128 v[200:203], v244 offset:41472\n"
    "v_exp_f32 v164, v164\n"
    "v_cvt_pk_bf16_f32 v209, v162, v163\n"
    "v_mul_f32 v8, v8, v224\n"
    "v_mul_f32 v24, v24, v224\n"
    "v_mfma_f32_32x32x16_bf16 v[112:127], v[212:215], a[124:127], v[112:127]\n"
    "ds_read_b128 v[204:207], v245 offset:41472\n"
    "v_exp_f32 v165, v165\n"
    "v_mul_f32 v40, v40, v224\n"
    "v_mul_f32 v56, v56, v224\n"
    "s_waitcnt lgkmcnt(2)\n"
    "v_mfma_f32_32x32x16_bf16 v[176:191], v[192:195], v[128:131], 0\n"
    "ds_read_b128 v[192:195], v244 offset:41984\n"
    "v_exp_f32 v166, v166\n"
    "v_cvt_pk_bf16_f32 v210, v164, v165\n"
    "v_add_f32 v242, v242, v162\n"
    "v_cvt_pk_bf16_f32 v232, v8, v24\n"
    "v_mfma_f32_32x32x16_bf16 v[176:191], v[196:199], v[132:135], v[176:191]\n"
    "ds_read_b128 v[196:199], v245 offset:41984\n"
    "v_exp_f32 v167, v167\n"
    "v_cvt_pk_bf16_f32 v233, v40, v56\n"
    "v_add_f32 v243, v243, v163\n"
    "s_waitcnt lgkmcnt(2)\n"
    "v_mfma_f32_32x32x16_bf16 v[176:191], v[200:203], v[136:139], v[176:191]\n"
    "ds_read_b128 v[200:203], v244 offset:42496\n"
    "v_exp_f32 v168, v168\n"
    "v_cvt_pk_bf16_f32 v211, v166, v167\n"
    "v_add_f32 v242, v242, v164\n"
    "v_add_f32 v243, v243, v165\n"
    "v_mfma_f32_32x32x16_bf16 v[176:191], v[204:207], v[140:143], v[176:191]\n"
    "ds_read_b128 v[204:207], v245 offset:42496\n"
    "v_exp_f32 v169, v169\n"
    "v_add_f32 v242, v242, v166\n"
    "v_add_f32 v243, v243, v167\n"
    "s_waitcnt lgkmcnt(2)\n"
    "v_mfma_f32_32x32x16_bf16 v[176:191], v[192:195], v[144:147], v[176:191]\n"
    "buffer_store_dwordx2 v[238:239], v250, s[48:51], s45 offen offset:2816 sc0 sc1\n"
    "v_exp_f32 v170, v170\n"
    "v_mfma_f32_32x32x16_bf16 v[176:191], v[196:199], v[148:151], v[176:191]\n"
    "buffer_load_dwordx4 v249, s[20:23], s41 offen offset:2176 nt lds\n"
    "v_exp_f32 v171, v171\n"
    "s_waitcnt lgkmcnt(0)\n"
    "v_mfma_f32_32x32x16_bf16 v[176:191], v[200:203], v[152:155], v[176:191]\n"
    "v_exp_f32 v172, v172\n"
    "s_add_u32 m0, s40, 36864\n"
    "v_cvt_pk_bf16_f32 v212, v168, v169\n"
    "v_cvt_pk_bf16_f32 v213, v170, v171\n"
    "v_add_f32 v242, v242, v168\n"
    "v_mfma_f32_32x32x16_bf16 v[176:191], v[204:207], v[156:159], v[176:191]\n"
    "buffer_load_dwordx4 v248, s[20:23], s42 offen offset:0 nt lds\n"
    "v_exp_f32 v173, v173\n"
    "v_add_f32 v243, v243, v169\n"
    "v_mfma_f32_32x32x16_bf16 v[64:79], v[208:211], a[128:131], v[64:79]\n"
    "buffer_load_dwordx4 v249, s[20:23], s42 offen offset:2048 nt lds\n"
    "v_exp_f32 v174, v174\n"
    "s_add_u32 m0, s40, 37760\n"
    "v_mfma_f32_32x32x16_bf16 v[80:95], v[208:211], a[132:135], v[80:95]\n"
    "buffer_load_dwordx4 v248, s[20:23], s42 offen offset:128 nt lds\n"
    "buffer_load_dwordx4 v249, s[20:23], s42 offen offset:2176 nt lds\n"
    "v_exp_f32 v175, v175\n"
    "v_mfma_f32_32x32x16_bf16 v[96:111], v[208:211], a[136:139], v[96:111]\n"
    "v_exp_f32 v176, v176\n"
    "v_cvt_pk_bf16_f32 v215, v174, v175\n"
    "v_cvt_pk_bf16_f32 v214, v172, v173\n"
    "v_add_f32 v242, v242, v170\n"
    "v_add_f32 v243, v243, v171\n"
    "v_mfma_f32_32x32x16_bf16 v[112:127], v[208:211], a[140:143], v[112:127]\n"
    "v_exp_f32 v177, v177\n"
    "v_add_f32 v242, v242, v172\n"
    "v_add_f32 v243, v243, v173\n"
    "v_add_f32 v242, v242, v174\n"
    "v_add_f32 v243, v243, v175\n"
    "v_mfma_f32_32x32x16_bf16 v[64:79], v[212:215], a[144:147], v[64:79]\n"
    "ds_read_b128 v[192:195], v244 offset:49152\n"
    "buffer_store_dwordx2 v[232:233], v250, s[24:27], s45 offen offset:0 sc0 sc1\n"
    "v_exp_f32 v178, v178\n"
    "v_mfma_f32_32x32x16_bf16 v[80:95], v[212:215], a[148:151], v[80:95]\n"
    "ds_read_b128 v[196:199], v245 offset:49152\n"
    "v_exp_f32 v179, v179\n"
    "v_mul_f32 v9, v9, v225\n"
    "v_mul_f32 v25, v25, v225\n"
    "v_mul_f32 v41, v41, v225\n"
    "v_mfma_f32_32x32x16_bf16 v[96:111], v[212:215], a[152:155], v[96:111]\n"
    "ds_read_b128 v[200:203], v244 offset:49664\n"
    "v_exp_f32 v180, v180\n"
    "v_mul_f32 v57, v57, v225\n"
    "v_cvt_pk_bf16_f32 v234, v9, v25\n"
    "v_cvt_pk_bf16_f32 v235, v41, v57\n"
    "v_mfma_f32_32x32x16_bf16 v[112:127], v[212:215], a[156:159], v[112:127]\n"
    "ds_read_b128 v[204:207], v245 offset:49664\n"
    "v_exp_f32 v181, v181\n"
    "v_cvt_pk_bf16_f32 v208, v176, v177\n"
    "v_cvt_pk_bf16_f32 v209, v178, v179\n"
    "s_waitcnt lgkmcnt(2)\n"
    "v_mfma_f32_32x32x16_bf16 v[160:175], v[192:195], v[128:131], 0\n"
    "ds_read_b128 v[192:195], v244 offset:50176\n"
    "v_exp_f32 v182, v182\n"
    "v_cvt_pk_bf16_f32 v210, v180, v181\n"
    "v_mul_f32 v10, v10, v226\n"
    "v_mul_f32 v26, v26, v226\n"
    "v_mfma_f32_32x32x16_bf16 v[160:175], v[196:199], v[132:135], v[160:175]\n"
    "ds_read_b128 v[196:199], v245 offset:50176\n"
    "v_exp_f32 v183, v183\n"
    "v_mul_f32 v42, v42, v226\n"
    "v_mul_f32 v58, v58, v226\n"
    "s_waitcnt lgkmcnt(2)\n"
    "v_mfma_f32_32x32x16_bf16 v[160:175], v[200:203], v[136:139], v[160:175]\n"
    "ds_read_b128 v[200:203], v244 offset:50688\n"
    "v_exp_f32 v184, v184\n"
    "v_cvt_pk_bf16_f32 v211, v182, v183\n"
    "v_cvt_pk_bf16_f32 v236, v10, v26\n"
    "v_cvt_pk_bf16_f32 v237, v42, v58\n"
    "v_mfma_f32_32x32x16_bf16 v[160:175], v[204:207], v[140:143], v[160:175]\n"
    "ds_read_b128 v[204:207], v245 offset:50688\n"
    "v_exp_f32 v185, v185\n"
    "v_add_f32 v242, v242, v176\n"
    "v_add_f32 v243, v243, v177\n"
    "s_waitcnt lgkmcnt(2)\n"
    "v_mfma_f32_32x32x16_bf16 v[160:175], v[192:195], v[144:147], v[160:175]\n"
    "buffer_store_dwordx2 v[234:235], v250, s[24:27], s45 offen offset:256 sc0 sc1\n"
    "v_exp_f32 v186, v186\n"
    "v_mfma_f32_32x32x16_bf16 v[160:175], v[196:199], v[148:151], v[160:175]\n"
    "v_exp_f32 v187, v187\n"
    "v_cvt_pk_bf16_f32 v212, v184, v185\n"
    "v_add_f32 v242, v242, v178\n"
    "v_add_f32 v243, v243, v179\n"
    "s_waitcnt lgkmcnt(0)\n"
    "v_mfma_f32_32x32x16_bf16 v[160:175], v[200:203], v[152:155], v[160:175]\n"
    "v_exp_f32 v188, v188\n"
    "v_cvt_pk_bf16_f32 v213, v186, v187\n"
    "v_add_f32 v242, v242, v180\n"
    "v_add_f32 v243, v243, v181\n"
    "v_add_f32 v242, v242, v182\n"
    "v_mfma_f32_32x32x16_bf16 v[160:175], v[204:207], v[156:159], v[160:175]\n"
    "buffer_store_dwordx2 v[236:237], v250, s[24:27], s45 offen offset:512 sc0 sc1\n"
    "v_exp_f32 v189, v189\n"
    "v_mfma_f32_32x32x16_bf16 v[64:79], v[208:211], a[160:163], v[64:79]\n"
    "v_exp_f32 v190, v190\n"
    "v_cvt_pk_bf16_f32 v214, v188, v189\n"
    "v_add_f32 v243, v243, v183\n"
    "v_add_f32 v242, v242, v184\n"
    "v_add_f32 v243, v243, v185\n"
    "v_mfma_f32_32x32x16_bf16 v[80:95], v[208:211], a[164:167], v[80:95]\n"
    "v_exp_f32 v191, v191\n"
    "v_add_f32 v242, v242, v186\n"
    "v_add_f32 v243, v243, v187\n"
    "v_add_f32 v242, v242, v188\n"
    "v_add_f32 v243, v243, v189\n"
    "v_mfma_f32_32x32x16_bf16 v[96:111], v[208:211], a[168:171], v[96:111]\n"
    "v_exp_f32 v160, v160\n"
    "v_cvt_pk_bf16_f32 v215, v190, v191\n"
    "v_add_f32 v242, v242, v190\n"
    "v_add_f32 v243, v243, v191\n"
    "v_mul_f32 v11, v11, v227\n"
    "v_mfma_f32_32x32x16_bf16 v[112:127], v[208:211], a[172:175], v[112:127]\n"
    "v_exp_f32 v161, v161\n"
    "v_mul_f32 v27, v27, v227\n"
    "v_mul_f32 v43, v43, v227\n"
    "v_mul_f32 v59, v59, v227\n"
    "v_cvt_pk_bf16_f32 v238, v11, v27\n"
    "v_mfma_f32_32x32x16_bf16 v[64:79], v[212:215], a[176:179], v[64:79]\n"
    "ds_read_b128 v[192:195], v244 offset:57344\n"
    "v_exp_f32 v162, v162\n"
    "v_cvt_pk_bf16_f32 v239, v43, v59\n"
    "v_mul_f32 v12, v12, v228\n"
    "v_mul_f32 v28, v28, v228\n"
    "v_mfma_f32_32x32x16_bf16 v[80:95], v[212:215], a[180:183], v[80:95]\n"
    "ds_read_b128 v[196:199], v245 offset:57344\n"
    "buffer_store_dwordx2 v[238:239], v250, s[24:27], s45 offen offset:768 sc0 sc1\n"
    "v_exp_f32 v163, v163\n"
    "v_mfma_f32_32x32x16_bf16 v[96:111], v[212:215], a[184:187], v[96:111]\n"
    "ds_read_b128 v[200:203], v244 offset:57856\n"
    "v_exp_f32 v164, v164\n"
    "v_mul_f32 v44, v44, v228\n"
    "v_mul_f32 v60, v60, v228\n"
    "v_cvt_pk_bf16_f32 v208, v160, v161\n"
    "v_mfma_f32_32x32x16_bf16 v[112:127], v[212:215], a[188:191], v[112:127]\n"
    "ds_read_b128 v[204:207], v245 offset:57856\n"
    "v_exp_f32 v165, v165\n"
    "v_cvt_pk_bf16_f32 v209, v162, v163\n"
    "v_cvt_pk_bf16_f32 v232, v12, v28\n"
    "s_waitcnt lgkmcnt(2)\n"
    "v_mfma_f32_32x32x16_bf16 v[176:191], v[192:195], v[128:131], 0\n"
    "ds_read_b128 v[192:195], v244 offset:58368\n"
    "s_waitcnt vmcnt(20)\n"
    "v_exp_f32 v166, v166\n"
    "v_cvt_pk_bf16_f32 v210, v164, v165\n"
    "v_cvt_pk_bf16_f32 v233, v44, v60\n"
    "v_mfma_f32_32x32x16_bf16 v[176:191], v[196:199], v[132:135], v[176:191]\n"
    "ds_read_b128 v[196:199], v245 offset:58368\n"
    "v_exp_f32 v167, v167\n"
    "v_add_f32 v242, v242, v160\n"
    "v_add_f32 v243, v243, v161\n"
    "s_waitcnt lgkmcnt(2)\n"
    "v_mfma_f32_32x32x16_bf16 v[176:191], v[200:203], v[136:139], v[176:191]\n"
    "ds_read_b128 v[200:203], v244 offset:58880\n"
    "v_exp_f32 v168, v168\n"
    "v_cvt_pk_bf16_f32 v211, v166, v167\n"
    "v_add_f32 v242, v242, v162\n"
    "v_add_f32 v243, v243, v163\n"
    "v_mfma_f32_32x32x16_bf16 v[176:191], v[204:207], v[140:143], v[176:191]\n"
    "ds_read_b128 v[204:207], v245 offset:58880\n"
    "v_exp_f32 v169, v169\n"
    "v_add_f32 v242, v242, v164\n"
    "v_add_f32 v243, v243, v165\n"
    "s_waitcnt lgkmcnt(2)\n"
    "v_mfma_f32_32x32x16_bf16 v[176:191], v[192:195], v[144:147], v[176:191]\n"
    "buffer_store_dwordx2 v[232:233], v250, s[24:27], s45 offen offset:2048 sc0 sc1\n"
    "v_exp_f32 v170, v170\n"
    "v_mfma_f32_32x32x16_bf16 v[176:191], v[196:199], v[148:151], v[176:191]\n"
    "v_exp_f32 v171, v171\n"
    "v_cvt_pk_bf16_f32 v212, v168, v169\n"
    "v_add_f32 v242, v242, v166\n"
    "v_add_f32 v243, v243, v167\n"
    "s_waitcnt lgkmcnt(0)\n"
    "v_mfma_f32_32x32x16_bf16 v[176:191], v[200:203], v[152:155], v[176:191]\n"
    "ds_read_b128 v[128:131], v246 offset:0\n"
    "v_exp_f32 v172, v172\n"
    "v_cvt_pk_bf16_f32 v213, v170, v171\n"
    "v_add_f32 v242, v242, v168\n"
    "v_add_f32 v243, v243, v169\n"
    "v_mfma_f32_32x32x16_bf16 v[176:191], v[204:207], v[156:159], v[176:191]\n"
    "ds_read_b128 v[132:135], v247 offset:0\n"
    "ds_read_b128 v[136:139], v246 offset:512\n"
    "ds_read_b128 v[140:143], v247 offset:512\n"
    "v_exp_f32 v173, v173\n"
    "v_add_f32 v242, v242, v170\n"
    "v_mfma_f32_32x32x16_bf16 v[64:79], v[208:211], a[192:195], v[64:79]\n"
    "ds_read_b128 v[144:147], v246 offset:1024\n"
    "ds_read_b128 v[148:151], v247 offset:1024\n"
    "v_exp_f32 v174, v174\n"
    "v_cvt_pk_bf16_f32 v214, v172, v173\n"
    "v_add_f32 v243, v243, v171\n"
    "v_mfma_f32_32x32x16_bf16 v[80:95], v[208:211], a[196:199], v[80:95]\n"
    "ds_read_b128 v[152:155], v246 offset:1536\n"
    "ds_read_b128 v[156:159], v247 offset:1536\n"
    "v_exp_f32 v175, v175\n"
    "v_add_f32 v242, v242, v172\n"
    "v_add_f32 v243, v243, v173\n"
    "v_mfma_f32_32x32x16_bf16 v[96:111], v[208:211], a[200:203], v[96:111]\n"
    "v_exp_f32 v176, v176\n"
    "v_cvt_pk_bf16_f32 v215, v174, v175\n"
    "v_add_f32 v242, v242, v174\n"
    "v_add_f32 v243, v243, v175\n"
    "v_mul_f32 v13, v13, v229\n"
    "v_mfma_f32_32x32x16_bf16 v[112:127], v[208:211], a[204:207], v[112:127]\n"
    "v_exp_f32 v177, v177\n"
    "v_mul_f32 v29, v29, v229\n"
    "v_mul_f32 v45, v45, v229\n"
    "v_mul_f32 v61, v61, v229\n"
    "v_cvt_pk_bf16_f32 v234, v13, v29\n"
    "s_cmp_eq_u32 s52, 3\n"
    "s_cbranch_scc1 Ldrain_%=\n"
    "v_mfma_f32_32x32x16_bf16 v[64:79], v[212:215], a[208:211], v[64:79]\n"
    "ds_read_b128 v[192:195], v244 offset:0\n"
    "v_exp_f32 v178, v178\n"
    "v_cvt_pk_bf16_f32 v235, v45, v61\n"
    "v_mul_f32 v14, v14, v230\n"
    "v_mul_f32 v30, v30, v230\n"
    "v_mfma_f32_32x32x16_bf16 v[80:95], v[212:215], a[212:215], v[80:95]\n"
    "ds_read_b128 v[196:199], v245 offset:0\n"
    "buffer_store_dwordx2 v[234:235], v250, s[24:27], s45 offen offset:2304 sc0 sc1\n"
    "v_exp_f32 v179, v179\n"
    "v_mfma_f32_32x32x16_bf16 v[96:111], v[212:215], a[216:219], v[96:111]\n"
    "ds_read_b128 v[200:203], v244 offset:512\n"
    "v_exp_f32 v180, v180\n"
    "v_mul_f32 v46, v46, v230\n"
    "v_mul_f32 v62, v62, v230\n"
    "v_cvt_pk_bf16_f32 v236, v14, v30\n"
    "v_mfma_f32_32x32x16_bf16 v[112:127], v[212:215], a[220:223], v[112:127]\n"
    "ds_read_b128 v[204:207], v245 offset:512\n"
    "v_exp_f32 v181, v181\n"
    "v_cvt_pk_bf16_f32 v237, v46, v62\n"
    "v_cvt_pk_bf16_f32 v208, v176, v177\n"
    "s_waitcnt lgkmcnt(2)\n"
    "v_mfma_f32_32x32x16_bf16 v[160:175], v[192:195], v[128:131], 0\n"
    "ds_read_b128 v[192:195], v244 offset:1024\n"
    "v_exp_f32 v182, v182\n"
    "v_cvt_pk_bf16_f32 v209, v178, v179\n"
    "v_cvt_pk_bf16_f32 v210, v180, v181\n"
    "v_mul_f32 v15, v15, v231\n"
    "v_mfma_f32_32x32x16_bf16 v[160:175], v[196:199], v[132:135], v[160:175]\n"
    "ds_read_b128 v[196:199], v245 offset:1024\n"
    "v_exp_f32 v183, v183\n"
    "v_mul_f32 v31, v31, v231\n"
    "v_mul_f32 v47, v47, v231\n"
    "s_waitcnt lgkmcnt(2)\n"
    "v_mfma_f32_32x32x16_bf16 v[160:175], v[200:203], v[136:139], v[160:175]\n"
    "ds_read_b128 v[200:203], v244 offset:1536\n"
    "v_exp_f32 v184, v184\n"
    "v_cvt_pk_bf16_f32 v211, v182, v183\n"
    "v_mul_f32 v63, v63, v231\n"
    "v_cvt_pk_bf16_f32 v238, v15, v31\n"
    "v_mfma_f32_32x32x16_bf16 v[160:175], v[204:207], v[140:143], v[160:175]\n"
    "ds_read_b128 v[204:207], v245 offset:1536\n"
    "v_exp_f32 v185, v185\n"
    "v_cvt_pk_bf16_f32 v239, v47, v63\n"
    "s_add_u32 s41, s41, 32768\n"
    "s_waitcnt lgkmcnt(2)\n"
    "v_mfma_f32_32x32x16_bf16 v[160:175], v[192:195], v[144:147], v[160:175]\n"
    "buffer_store_dwordx2 v[236:237], v250, s[24:27], s45 offen offset:2560 sc0 sc1\n"
    "v_exp_f32 v186, v186\n"
    "v_mfma_f32_32x32x16_bf16 v[160:175], v[196:199], v[148:151], v[160:175]\n"
    "v_exp_f32 v187, v187\n"
    "v_cvt_pk_bf16_f32 v212, v184, v185\n"
    "s_add_u32 s42, s42, 32768\n"
    "v_add_f32 v242, v242, v176\n"
    "s_waitcnt lgkmcnt(0)\n"
    "v_mfma_f32_32x32x16_bf16 v[160:175], v[200:203], v[152:155], v[160:175]\n"
    "v_exp_f32 v188, v188\n"
    "v_cvt_pk_bf16_f32 v213, v186, v187\n"
    "v_add_f32 v243, v243, v177\n"
    "v_add_f32 v242, v242, v178\n"
    "v_add_f32 v243, v243, v179\n"
    "v_mfma_f32_32x32x16_bf16 v[160:175], v[204:207], v[156:159], v[160:175]\n"
    "buffer_store_dwordx2 v[238:239], v250, s[24:27], s45 offen offset:2816 sc0 sc1\n"
    "v_exp_f32 v189, v189\n"
    "v_mfma_f32_32x32x16_bf16 v[64:79], v[208:211], a[224:227], v[64:79]\n"
    "v_exp_f32 v190, v190\n"
    "v_cvt_pk_bf16_f32 v214, v188, v189\n"
    "v_add_f32 v242, v242, v180\n"
    "v_add_f32 v243, v243, v181\n"
    "v_add_f32 v242, v242, v182\n"
    "v_mfma_f32_32x32x16_bf16 v[80:95], v[208:211], a[228:231], v[80:95]\n"
    "v_exp_f32 v191, v191\n"
    "v_add_f32 v243, v243, v183\n"
    "v_add_f32 v242, v242, v184\n"
    "v_add_f32 v243, v243, v185\n"
    "v_add_f32 v242, v242, v186\n"
    "v_mfma_f32_32x32x16_bf16 v[96:111], v[208:211], a[232:235], v[96:111]\n"
    "v_exp_f32 v160, v160\n"
    "v_cvt_pk_bf16_f32 v215, v190, v191\n"
    "v_add_f32 v243, v243, v187\n"
    "v_add_f32 v242, v242, v188\n"
    "v_add_f32 v243, v243, v189\n"
    "v_mfma_f32_32x32x16_bf16 v[112:127], v[208:211], a[236:239], v[112:127]\n"
    "v_exp_f32 v161, v161\n"
    "v_add_f32 v242, v242, v190\n"
    "v_add_f32 v243, v243, v191\n"
    "v_mfma_f32_32x32x16_bf16 v[64:79], v[212:215], a[240:243], v[64:79]\n"
    "ds_read_b128 v[192:195], v244 offset:8192\n"
    "v_exp_f32 v162, v162\n"
    "v_add_f32 v242, v242, v243\n"
    "v_mov_b32 v255, v242\n"
    "v_add_f32 v240, v160, v161\n"
    "v_mfma_f32_32x32x16_bf16 v[80:95], v[212:215], a[244:247], v[80:95]\n"
    "ds_read_b128 v[196:199], v245 offset:8192\n"
    "v_exp_f32 v163, v163\n"
    "v_permlane32_swap_b32 v242, v255\n"
    "v_add_f32 v242, v242, v255\n"
    "s_add_u32 s45, s45, 32768\n"
    "v_mfma_f32_32x32x16_bf16 v[96:111], v[212:215], a[248:251], v[96:111]\n"
    "ds_read_b128 v[200:203], v244 offset:8704\n"
    "v_exp_f32 v164, v164\n"
    "v_add_f32 v241, v162, v163\n"
    "v_rcp_f32 v255, v242\n"
    "v_mfma_f32_32x32x16_bf16 v[112:127], v[212:215], a[252:255], v[112:127]\n"
    "ds_read_b128 v[204:207], v245 offset:8704\n"
    "ds_write_b32 v251, v255\n"
    "v_exp_f32 v165, v165\n"
    "s_waitcnt lgkmcnt(3)\n"
    "v_mfma_f32_32x32x16_bf16 v[176:191], v[192:195], v[128:131], 0\n"
    "ds_read_b128 v[192:195], v244 offset:9216\n"
    "ds_read_b128 v[216:219], v252 offset:0\n"
    "v_exp_f32 v166, v166\n"
    "s_cmp_ge_u32 s52, 2\n"
    "s_cselect_b32 s22, 0, 0x40000\n"
    "v_mfma_f32_32x32x16_bf16 v[176:191], v[196:199], v[132:135], v[176:191]\n"
    "ds_read_b128 v[196:199], v245 offset:9216\n"
    "ds_read_b128 v[220:223], v252 offset:32\n"
    "v_exp_f32 v167, v167\n"
    "v_cvt_pk_bf16_f32 v208, v160, v161\n"
    "s_waitcnt lgkmcnt(5)\n"
    "v_mfma_f32_32x32x16_bf16 v[176:191], v[200:203], v[136:139], v[176:191]\n"
    "ds_read_b128 v[200:203], v244 offset:9728\n"
    "ds_read_b128 v[224:227], v252 offset:64\n"
    "v_exp_f32 v168, v168\n"
    "v_cvt_pk_bf16_f32 v209, v162, v163\n"
    "v_cvt_pk_bf16_f32 v210, v164, v165\n"
    "v_mfma_f32_32x32x16_bf16 v[176:191], v[204:207], v[140:143], v[176:191]\n"
    "ds_read_b128 v[204:207], v245 offset:9728\n"
    "ds_read_b128 v[228:231], v252 offset:96\n"
    "v_exp_f32 v169, v169\n"
    "v_cvt_pk_bf16_f32 v211, v166, v167\n"
    "s_waitcnt lgkmcnt(5)\n"
    "v_mfma_f32_32x32x16_bf16 v[176:191], v[192:195], v[144:147], v[176:191]\n"
    "v_exp_f32 v170, v170\n"
    "v_cvt_pk_bf16_f32 v212, v168, v169\n"
    "v_max3_f32 v253, v253, v242, v255\n"
    "v_add_f32 v240, v240, v164\n"
    "v_add_f32 v241, v241, v165\n"
    "v_mfma_f32_32x32x16_bf16 v[176:191], v[196:199], v[148:151], v[176:191]\n"
    "v_exp_f32 v171, v171\n"
    "v_add_f32 v240, v240, v166\n"
    "v_add_f32 v241, v241, v167\n"
    "v_add_f32 v240, v240, v168\n"
    "s_waitcnt lgkmcnt(1)\n"
    "v_mfma_f32_32x32x16_bf16 v[176:191], v[200:203], v[152:155], v[176:191]\n"
    "v_exp_f32 v172, v172\n"
    "v_cvt_pk_bf16_f32 v213, v170, v171\n"
    "v_add_f32 v241, v241, v169\n"
    "v_add_f32 v240, v240, v170\n"
    "v_add_f32 v241, v241, v171\n"
    "v_mfma_f32_32x32x16_bf16 v[176:191], v[204:207], v[156:159], v[176:191]\n"
    "v_exp_f32 v173, v173\n"
    "v_add_f32 v240, v240, v172\n"
    "v_mul_f32 v64, v64, v216\n"
    "v_mul_f32 v80, v80, v216\n"
    "v_mul_f32 v96, v96, v216\n"
    "v_mfma_f32_32x32x16_bf16 v[0:15], v[208:211], a[0:3], 0\n"
    "v_exp_f32 v174, v174\n"
    "v_cvt_pk_bf16_f32 v214, v172, v173\n"
    "v_add_f32 v241, v241, v173\n"
    "v_mul_f32 v112, v112, v216\n"
    "v_cvt_pk_bf16_f32 v232, v64, v80\n"
    "v_mfma_f32_32x32x16_bf16 v[16:31], v[208:211], a[4:7], 0\n"
    "v_exp_f32 v175, v175\n"
    "v_add_f32 v240, v240, v174\n"
    "v_cvt_pk_bf16_f32 v233, v96, v112\n"
    "s_add_u32 m0, s40, 0\n"
    "v_mfma_f32_32x32x16_bf16 v[32:47], v[208:211], a[8:11], 0\n"
    "v_exp_f32 v176, v176\n"
    "v_cvt_pk_bf16_f32 v215, v174, v175\n"
    "v_add_f32 v241, v241, v175\n"
    "v_mfma_f32_32x32x16_bf16 v[48:63], v[208:211], a[12:15], 0\n"
    "buffer_store_dwordx2 v[232:233], v250, s[48:51], s45 offen offset:0 sc0 sc1\n"
    "v_exp_f32 v177, v177\n"
    "v_mfma_f32_32x32x16_bf16 v[0:15], v[212:215], a[16:19], v[0:15]\n"
    "ds_read_b128 v[192:195], v244 offset:16384\n"
    "v_exp_f32 v178, v178\n"
    "v_cvt_pk_bf16_f32 v208, v176, v177\n"
    "v_add_f32 v240, v240, v176\n"
    "v_add_f32 v241, v241, v177\n"
    "v_mfma_f32_32x32x16_bf16 v[16:31], v[212:215], a[20:23], v[16:31]\n"
    "ds_read_b128 v[196:199], v245 offset:16384\n"
    "v_exp_f32 v179, v179\n"
    "v_mul_f32 v65, v65, v217\n"
    "v_mul_f32 v81, v81, v217\n"
    "v_mul_f32 v97, v97, v217\n"
    "v_mfma_f32_32x32x16_bf16 v[32:47], v[212:215], a[24:27], v[32:47]\n"
    "ds_read_b128 v[200:203], v244 offset:16896\n"
    "v_exp_f32 v180, v180\n"
    "v_cvt_pk_bf16_f32 v209, v178, v179\n"
    "v_mul_f32 v113, v113, v217\n"
    "v_cvt_pk_bf16_f32 v234, v65, v81\n"
    "v_mfma_f32_32x32x16_bf16 v[48:63], v[212:215], a[28:31], v[48:63]\n"
    "ds_read_b128 v[204:207], v245 offset:16896\n"
    "v_exp_f32 v181, v181\n"
    "v_cvt_pk_bf16_f32 v235, v97, v113\n"
    "v_add_f32 v240, v240, v178\n"
    "s_waitcnt lgkmcnt(2)\n"
    "v_mfma_f32_32x32x16_bf16 v[160:175], v[192:195], v[128:131], 0\n"
    "ds_read_b128 v[192:195], v244 offset:17408\n"
    "v_exp_f32 v182, v182\n"
    "v_cvt_pk_bf16_f32 v210, v180, v181\n"
    "v_add_f32 v241, v241, v179\n"
    "v_add_f32 v240, v240, v180\n"
    "v_mfma_f32_32x32x16_bf16 v[160:175], v[196:199], v[132:135], v[160:175]\n"
    "ds_read_b128 v[196:199], v245 offset:17408\n"
    "v_exp_f32 v183, v183\n"
    "v_add_f32 v241, v241, v181\n"
    "v_add_f32 v240, v240, v182\n"
    "s_waitcnt lgkmcnt(2)\n"
    "v_mfma_f32_32x32x16_bf16 v[160:175], v[200:203], v[136:139], v[160:175]\n"
    "ds_read_b128 v[200:203], v244 offset:17920\n"
    "v_exp_f32 v184, v184\n"
    "v_cvt_pk_bf16_f32 v211, v182, v183\n"
    "v_add_f32 v241, v241, v183\n"
    "v_mfma_f32_32x32x16_bf16 v[160:175], v[204:207], v[140:143], v[160:175]\n"
    "ds_read_b128 v[204:207], v245 offset:17920\n"
    "v_exp_f32 v185, v185\n"
    "v_add_f32 v240, v240, v184\n"
    "s_waitcnt lgkmcnt(2)\n"
    "v_mfma_f32_32x32x16_bf16 v[160:175], v[192:195], v[144:147], v[160:175]\n"
    "v_exp_f32 v186, v186\n"
    "v_cvt_pk_bf16_f32 v212, v184, v185\n"
    "v_add_f32 v241, v241, v185\n"
    "v_mul_f32 v66, v66, v218\n"
    "v_mul_f32 v82, v82, v218\n"
    "v_mfma_f32_32x32x16_bf16 v[160:175], v[196:199], v[148:151], v[160:175]\n"
    "v_exp_f32 v187, v187\n"
    "v_add_f32 v240, v240, v186\n"
    "v_mul_f32 v98, v98, v218\n"
    "v_mul_f32 v114, v114, v218\n"
    "s_waitcnt lgkmcnt(0)\n"
    "v_mfma_f32_32x32x16_bf16 v[160:175], v[200:203], v[152:155], v[160:175]\n"
    "v_exp_f32 v188, v188\n"
    "v_cvt_pk_bf16_f32 v213, v186, v187\n"
    "v_add_f32 v241, v241, v187\n"
    "v_cvt_pk_bf16_f32 v236, v66, v82\n"
    "v_cvt_pk_bf16_f32 v237, v98, v114\n"
    "v_mfma_f32_32x32x16_bf16 v[160:175], v[204:207], v[156:159], v[160:175]\n"
    "buffer_load_dwordx4 v248, s[20:23], s41 offen offset:0 nt lds\n"
    "v_exp_f32 v189, v189\n"
    "v_add_f32 v240, v240, v188\n"
    "v_mfma_f32_32x32x16_bf16 v[0:15], v[208:211], a[32:35], v[0:15]\n"
    "v_exp_f32 v190, v190\n"
    "v_cvt_pk_bf16_f32 v214, v188, v189\n"
    "v_add_f32 v241, v241, v189\n"
    "v_mfma_f32_32x32x16_bf16 v[16:31], v[208:211], a[36:39], v[16:31]\n"
    "buffer_load_dwordx4 v249, s[20:23], s41 offen offset:2048 nt lds\n"
    "v_exp_f32 v191, v191\n"
    "v_add_f32 v240, v240, v190\n"
    "v_mfma_f32_32x32x16_bf16 v[32:47], v[208:211], a[40:43], v[32:47]\n"
    "v_exp_f32 v160, v160\n"
    "v_cvt_pk_bf16_f32 v215, v190, v191\n"
    "v_add_f32 v241, v241, v191\n"
    "s_add_u32 m0, s40, 896\n"
    "v_mfma_f32_32x32x16_bf16 v[48:63], v[208:211], a[44:47], v[48:63]\n"
    "buffer_store_dwordx2 v[234:235], v250, s[48:51], s45 offen offset:256 sc0 sc1\n"
    "v_exp_f32 v161, v161\n"
    "v_mfma_f32_32x32x16_bf16 v[0:15], v[212:215], a[48:51], v[0:15]\n"
    "ds_read_b128 v[192:195], v244 offset:24576\n"
    "buffer_store_dwordx2 v[236:237], v250, s[48:51], s45 offen offset:512 sc0 sc1\n"
    "v_exp_f32 v162, v162\n"
    "v_mfma_f32_32x32x16_bf16 v[16:31], v[212:215], a[52:55], v[16:31]\n"
    "ds_read_b128 v[196:199], v245 offset:24576\n"
    "v_exp_f32 v163, v163\n"
    "v_cvt_pk_bf16_f32 v208, v160, v161\n"
    "v_mul_f32 v67, v67, v219\n"
    "v_mul_f32 v83, v83, v219\n"
    "v_mfma_f32_32x32x16_bf16 v[32:47], v[212:215], a[56:59], v[32:47]\n"
    "ds_read_b128 v[200:203], v244 offset:25088\n"
    "v_exp_f32 v164, v164\n"
    "v_cvt_pk_bf16_f32 v209, v162, v163\n"
    "v_mul_f32 v99, v99, v219\n"
    "v_mul_f32 v115, v115, v219\n"
    "v_mfma_f32_32x32x16_bf16 v[48:63], v[212:215], a[60:63], v[48:63]\n"
    "ds_read_b128 v[204:207], v245 offset:25088\n"
    "v_exp_f32 v165, v165\n"
    "v_cvt_pk_bf16_f32 v238, v67, v83\n"
    "v_cvt_pk_bf16_f32 v239, v99, v115\n"
    "s_waitcnt lgkmcnt(2)\n"
    "v_mfma_f32_32x32x16_bf16 v[176:191], v[192:195], v[128:131], 0\n"
    "ds_read_b128 v[192:195], v244 offset:25600\n"
    "v_exp_f32 v166, v166\n"
    "v_cvt_pk_bf16_f32 v210, v164, v165\n"
    "v_add_f32 v240, v240, v160\n"
    "v_add_f32 v241, v241, v161\n"
    "v_mfma_f32_32x32x16_bf16 v[176:191], v[196:199], v[132:135], v[176:191]\n"
    "ds_read_b128 v[196:199], v245 offset:25600\n"
    "v_exp_f32 v167, v167\n"
    "v_add_f32 v240, v240, v162\n"
    "v_add_f32 v241, v241, v163\n"
    "s_waitcnt lgkmcnt(2)\n"
    "v_mfma_f32_32x32x16_bf16 v[176:191], v[200:203], v[136:139], v[176:191]\n"
    "ds_read_b128 v[200:203], v244 offset:26112\n"
    "v_exp_f32 v168, v168\n"
    "v_cvt_pk_bf16_f32 v211, v166, v167\n"
    "v_add_f32 v240, v240, v164\n"
    "v_add_f32 v241, v241, v165\n"
    "v_mfma_f32_32x32x16_bf16 v[176:191], v[204:207], v[140:143], v[176:191]\n"
    "ds_read_b128 v[204:207], v245 offset:26112\n"
    "v_exp_f32 v169, v169\n"
    "v_add_f32 v240, v240, v166\n"
    "v_add_f32 v241, v241, v167\n"
    "s_waitcnt lgkmcnt(2)\n"
    "v_mfma_f32_32x32x16_bf16 v[176:191], v[192:195], v[144:147], v[176:191]\n"
    "buffer_store_dwordx2 v[238:239], v250, s[48:51], s45 offen offset:768 sc0 sc1\n"
    "v_exp_f32 v170, v170\n"
    "v_mfma_f32_32x32x16_bf16 v[176:191], v[196:199], v[148:151], v[176:191]\n"
    "v_exp_f32 v171, v171\n"
    "v_cvt_pk_bf16_f32 v212, v168, v169\n"
    "v_add_f32 v240, v240, v168\n"
    "v_add_f32 v241, v241, v169\n"
    "s_waitcnt lgkmcnt(0)\n"
    "v_mfma_f32_32x32x16_bf16 v[176:191], v[200:203], v[152:155], v[176:191]\n"
    "v_exp_f32 v172, v172\n"
    "v_cvt_pk_bf16_f32 v213, v170, v171\n"
    "v_add_f32 v240, v240, v170\n"
    "v_add_f32 v241, v241, v171\n"
    "v_mul_f32 v68, v68, v220\n"
    "v_mfma_f32_32x32x16_bf16 v[176:191], v[204:207], v[156:159], v[176:191]\n"
    "v_exp_f32 v173, v173\n"
    "v_add_f32 v240, v240, v172\n"
    "v_mul_f32 v84, v84, v220\n"
    "v_mul_f32 v100, v100, v220\n"
    "v_mul_f32 v116, v116, v220\n"
    "v_mfma_f32_32x32x16_bf16 v[0:15], v[208:211], a[64:67], v[0:15]\n"
    "v_exp_f32 v174, v174\n"
    "v_cvt_pk_bf16_f32 v214, v172, v173\n"
    "v_add_f32 v241, v241, v173\n"
    "v_cvt_pk_bf16_f32 v232, v68, v84\n"
    "v_cvt_pk_bf16_f32 v233, v100, v116\n"
    "v_mfma_f32_32x32x16_bf16 v[16:31], v[208:211], a[68:71], v[16:31]\n"
    "v_exp_f32 v175, v175\n"
    "v_add_f32 v240, v240, v174\n"
    "v_mul_f32 v69, v69, v221\n"
    "v_mul_f32 v85, v85, v221\n"
    "v_mul_f32 v101, v101, v221\n"
    "v_mfma_f32_32x32x16_bf16 v[32:47], v[208:211], a[72:75], v[32:47]\n"
    "v_exp_f32 v176, v176\n"
    "v_cvt_pk_bf16_f32 v215, v174, v175\n"
    "v_add_f32 v241, v241, v175\n"
    "v_mul_f32 v117, v117, v221\n"
    "v_cvt_pk_bf16_f32 v234, v69, v85\n"
    "v_mfma_f32_32x32x16_bf16 v[48:63], v[208:211], a[76:79], v[48:63]\n"
    "buffer_store_dwordx2 v[232:233], v250, s[48:51], s45 offen offset:2048 sc0 sc1\n"
    "v_exp_f32 v177, v177\n"
    "v_mfma_f32_32x32x16_bf16 v[0:15], v[212:215], a[80:83], v[0:15]\n"
    "ds_read_b128 v[192:195], v244 offset:32768\n"
    "v_exp_f32 v178, v178\n"
    "v_cvt_pk_bf16_f32 v235, v101, v117\n"
    "v_cvt_pk_bf16_f32 v208, v176, v177\n"
    "v_add_f32 v240, v240, v176\n"
    "v_mfma_f32_32x32x16_bf16 v[16:31], v[212:215], a[84:87], v[16:31]\n"
    "ds_read_b128 v[196:199], v245 offset:32768\n"
    "v_exp_f32 v179, v179\n"
    "v_add_f32 v241, v241, v177\n"
    "v_add_f32 v240, v240, v178\n"
    "v_mfma_f32_32x32x16_bf16 v[32:47], v[212:215], a[88:91], v[32:47]\n"
    "ds_read_b128 v[200:203], v244 offset:33280\n"
    "v_exp_f32 v180, v180\n"
    "v_cvt_pk_bf16_f32 v209, v178, v179\n"
    "v_add_f32 v241, v241, v179\n"
    "v_mfma_f32_32x32x16_bf16 v[48:63], v[212:215], a[92:95], v[48:63]\n"
    "ds_read_b128 v[204:207], v245 offset:33280\n"
    "v_exp_f32 v181, v181\n"
    "v_add_f32 v240, v240, v180\n"
    "s_waitcnt lgkmcnt(2)\n"
    "v_mfma_f32_32x32x16_bf16 v[160:175], v[192:195], v[128:131], 0\n"
    "ds_read_b128 v[192:195], v244 offset:33792\n"
    "v_exp_f32 v182, v182\n"
    "v_cvt_pk_bf16_f32 v210, v180, v181\n"
    "v_add_f32 v241, v241, v181\n"
    "v_mul_f32 v70, v70, v222\n"
    "v_mfma_f32_32x32x16_bf16 v[160:175], v[196:199], v[132:135], v[160:175]\n"
    "ds_read_b128 v[196:199], v245 offset:33792\n"
    "v_exp_f32 v183, v183\n"
    "v_add_f32 v240, v240, v182\n"
    "v_mul_f32 v86, v86, v222\n"
    "s_waitcnt lgkmcnt(2)\n"
    "v_mfma_f32_32x32x16_bf16 v[160:175], v[200:203], v[136:139], v[160:175]\n"
    "ds_read_b128 v[200:203], v244 offset:34304\n"
    "v_exp_f32 v184, v184\n"
    "v_cvt_pk_bf16_f32 v211, v182, v183\n"
    "v_add_f32 v241, v241, v183\n"
    "v_mul_f32 v102, v102, v222\n"
    "v_mfma_f32_32x32x16_bf16 v[160:175], v[204:207], v[140:143], v[160:175]\n"
    "ds_read_b128 v[204:207], v245 offset:34304\n"
    "v_exp_f32 v185, v185\n"
    "v_add_f32 v240, v240, v184\n"
    "v_mul_f32 v118, v118, v222\n"
    "s_waitcnt lgkmcnt(2)\n"
    "v_mfma_f32_32x32x16_bf16 v[160:175], v[192:195], v[144:147], v[160:175]\n"
    "buffer_store_dwordx2 v[234:235], v250, s[48:51], s45 offen offset:2304 sc0 sc1\n"
    "v_exp_f32 v186, v186\n"
    "v_mfma_f32_32x32x16_bf16 v[160:175], v[196:199], v[148:151], v[160:175]\n"
    "v_exp_f32 v187, v187\n"
    "v_cvt_pk_bf16_f32 v212, v184, v185\n"
    "v_add_f32 v241, v241, v185\n"
    "v_add_f32 v240, v240, v186\n"
    "s_waitcnt lgkmcnt(0)\n"
    "v_mfma_f32_32x32x16_bf16 v[160:175], v[200:203], v[152:155], v[160:175]\n"
    "v_exp_f32 v188, v188\n"
    "v_cvt_pk_bf16_f32 v213, v186, v187\n"
    "v_add_f32 v241, v241, v187\n"
    "v_cvt_pk_bf16_f32 v236, v70, v86\n"
    "v_cvt_pk_bf16_f32 v237, v102, v118\n"
    "v_mfma_f32_32x32x16_bf16 v[160:175], v[204:207], v[156:159], v[160:175]\n"
    "v_exp_f32 v189, v189\n"
    "v_add_f32 v240, v240, v188\n"
    "v_mul_f32 v71, v71, v223\n"
    "v_mul_f32 v87, v87, v223\n"
    "v_mul_f32 v103, v103, v223\n"
    "v_mfma_f32_32x32x16_bf16 v[0:15], v[208:211], a[96:99], v[0:15]\n"
    "v_exp_f32 v190, v190\n"
    "v_cvt_pk_bf16_f32 v214, v188, v189\n"
    "v_add_f32 v241, v241, v189\n"
    "v_mul_f32 v119, v119, v223\n"
    "v_cvt_pk_bf16_f32 v238, v71, v87\n"
    "v_mfma_f32_32x32x16_bf16 v[16:31], v[208:211], a[100:103], v[16:31]\n"
    "v_exp_f32 v191, v191\n"
    "v_add_f32 v240, v240, v190\n"
    "v_cvt_pk_bf16_f32 v239, v103, v119\n"
    "v_mfma_f32_32x32x16_bf16 v[32:47], v[208:211], a[104:107], v[32:47]\n"
    "v_exp_f32 v160, v160\n"
    "v_cvt_pk_bf16_f32 v215, v190, v191\n"
    "v_add_f32 v241, v241, v191\n"
    "v_mfma_f32_32x32x16_bf16 v[48:63], v[208:211], a[108:111], v[48:63]\n"
    "buffer_store_dwordx2 v[236:237], v250, s[48:51], s45 offen offset:2560 sc0 sc1\n"
    "v_exp_f32 v161, v161\n"
    "v_mfma_f32_32x32x16_bf16 v[0:15], v[212:215], a[112:115], v[0:15]\n"
    "ds_read_b128 v[192:195], v244 offset:40960\n"
    "v_exp_f32 v162, v162\n"
    "v_cvt_pk_bf16_f32 v208, v160, v161\n"
    "v_add_f32 v240, v240, v160\n"
    "v_add_f32 v241, v241, v161\n"
    "v_mfma_f32_32x32x16_bf16 v[16:31], v[212:215], a[116:119], v[16:31]\n"
    "ds_read_b128 v[196:199], v245 offset:40960\n"
    "buffer_load_dwordx4 v248, s[20:23], s41 offen offset:128 nt lds\n"
    "v_exp_f32 v163, v163\n"
    "v_mfma_f32_32x32x16_bf16 v[32:47], v[212:215], a[120:123], v[32:47]\n"
    "ds_read_b128 v[200:203], v244 offset:41472\n"
    "v_exp_f32 v164, v164\n"
    "v_cvt_pk_bf16_f32 v209, v162, v163\n"
    "v_mul_f32 v72, v72, v224\n"
    "v_mul_f32 v88, v88, v224\n"
    "v_mfma_f32_32x32x16_bf16 v[48:63], v[212:215], a[124:127], v[48:63]\n"
    "ds_read_b128 v[204:207], v245 offset:41472\n"
    "v_exp_f32 v165, v165\n"
    "v_mul_f32 v104, v104, v224\n"
    "v_mul_f32 v120, v120, v224\n"
    "s_waitcnt lgkmcnt(2)\n"
    "v_mfma_f32_32x32x16_bf16 v[176:191], v[192:195], v[128:131], 0\n"
    "ds_read_b128 v[192:195], v244 offset:41984\n"
    "v_exp_f32 v166, v166\n"
    "v_cvt_pk_bf16_f32 v210, v164, v165\n"
    "v_add_f32 v240, v240, v162\n"
    "v_cvt_pk_bf16_f32 v232, v72, v88\n"
    "v_mfma_f32_32x32x16_bf16 v[176:191], v[196:199], v[132:135], v[176:191]\n"
    "ds_read_b128 v[196:199], v245 offset:41984\n"
    "v_exp_f32 v167, v167\n"
    "v_cvt_pk_bf16_f32 v233, v104, v120\n"
    "v_add_f32 v241, v241, v163\n"
    "s_waitcnt lgkmcnt(2)\n"
    "v_mfma_f32_32x32x16_bf16 v[176:191], v[200:203], v[136:139], v[176:191]\n"
    "ds_read_b128 v[200:203], v244 offset:42496\n"
    "v_exp_f32 v168, v168\n"
    "v_cvt_pk_bf16_f32 v211, v166, v167\n"
    "v_add_f32 v240, v240, v164\n"
    "v_add_f32 v241, v241, v165\n"
    "v_mfma_f32_32x32x16_bf16 v[176:191], v[204:207], v[140:143], v[176:191]\n"
    "ds_read_b128 v[204:207], v245 offset:42496\n"
    "v_exp_f32 v169, v169\n"
    "v_add_f32 v240, v240, v166\n"
    "v_add_f32 v241, v241, v167\n"
    "s_waitcnt lgkmcnt(2)\n"
    "v_mfma_f32_32x32x16_bf16 v[176:191], v[192:195], v[144:147], v[176:191]\n"
    "buffer_store_dwordx2 v[238:239], v250, s[48:51], s45 offen offset:2816 sc0 sc1\n"
    "v_exp_f32 v170, v170\n"
    "v_mfma_f32_32x32x16_bf16 v[176:191], v[196:199], v[148:151], v[176:191]\n"
    "buffer_load_dwordx4 v249, s[20:23], s41 offen offset:2176 nt lds\n"
    "v_exp_f32 v171, v171\n"
    "s_waitcnt lgkmcnt(0)\n"
    "v_mfma_f32_32x32x16_bf16 v[176:191], v[200:203], v[152:155], v[176:191]\n"
    "v_exp_f32 v172, v172\n"
    "s_add_u32 m0, s40, 4096\n"
    "v_cvt_pk_bf16_f32 v212, v168, v169\n"
    "v_cvt_pk_bf16_f32 v213, v170, v171\n"
    "v_add_f32 v240, v240, v168\n"
    "v_mfma_f32_32x32x16_bf16 v[176:191], v[204:207], v[156:159], v[176:191]\n"
    "buffer_load_dwordx4 v248, s[20:23], s42 offen offset:0 nt lds\n"
    "v_exp_f32 v173, v173\n"
    "v_add_f32 v241, v241, v169\n"
    "v_mfma_f32_32x32x16_bf16 v[0:15], v[208:211], a[128:131], v[0:15]\n"
    "buffer_load_dwordx4 v249, s[20:23], s42 offen offset:2048 nt lds\n"
    "v_exp_f32 v174, v174\n"
    "s_add_u32 m0, s40, 4992\n"
    "v_mfma_f32_32x32x16_bf16 v[16:31], v[208:211], a[132:135], v[16:31]\n"
    "buffer_load_dwordx4 v248, s[20:23], s42 offen offset:128 nt lds\n"
    "buffer_load_dwordx4 v249, s[20:23], s42 offen offset:2176 nt lds\n"
    "v_exp_f32 v175, v175\n"
    "v_mfma_f32_32x32x16_bf16 v[32:47], v[208:211], a[136:139], v[32:47]\n"
    "v_exp_f32 v176, v176\n"
    "v_cvt_pk_bf16_f32 v215, v174, v175\n"
    "v_cvt_pk_bf16_f32 v214, v172, v173\n"
    "v_add_f32 v240, v240, v170\n"
    "v_add_f32 v241, v241, v171\n"
    "v_mfma_f32_32x32x16_bf16 v[48:63], v[208:211], a[140:143], v[48:63]\n"
    "v_exp_f32 v177, v177\n"
    "v_add_f32 v240, v240, v172\n"
    "v_add_f32 v241, v241, v173\n"
    "v_add_f32 v240, v240, v174\n"
    "v_add_f32 v241, v241, v175\n"
    "v_mfma_f32_32x32x16_bf16 v[0:15], v[212:215], a[144:147], v[0:15]\n"
    "ds_read_b128 v[192:195], v244 offset:49152\n"
    "buffer_store_dwordx2 v[232:233], v250, s[24:27], s45 offen offset:0 sc0 sc1\n"
    "v_exp_f32 v178, v178\n"
    "v_mfma_f32_32x32x16_bf16 v[16:31], v[212:215], a[148:151], v[16:31]\n"
    "ds_read_b128 v[196:199], v245 offset:49152\n"
    "v_exp_f32 v179, v179\n"
    "v_mul_f32 v73, v73, v225\n"
    "v_mul_f32 v89, v89, v225\n"
    "v_mul_f32 v105, v105, v225\n"
    "v_mfma_f32_32x32x16_bf16 v[32:47], v[212:215], a[152:155], v[32:47]\n"
    "ds_read_b128 v[200:203], v244 offset:49664\n"
    "v_exp_f32 v180, v180\n"
    "v_mul_f32 v121, v121, v225\n"
    "v_cvt_pk_bf16_f32 v234, v73, v89\n"
    "v_cvt_pk_bf16_f32 v235, v105, v121\n"
    "v_mfma_f32_32x32x16_bf16 v[48:63], v[212:215], a[156:159], v[48:63]\n"
    "ds_read_b128 v[204:207], v245 offset:49664\n"
    "v_exp_f32 v181, v181\n"
    "v_cvt_pk_bf16_f32 v208, v176, v177\n"
    "v_cvt_pk_bf16_f32 v209, v178, v179\n"
    "s_waitcnt lgkmcnt(2)\n"
    "v_mfma_f32_32x32x16_bf16 v[160:175], v[192:195], v[128:131], 0\n"
    "ds_read_b128 v[192:195], v244 offset:50176\n"
    "v_exp_f32 v182, v182\n"
    "v_cvt_pk_bf16_f32 v210, v180, v181\n"
    "v_mul_f32 v74, v74, v226\n"
    "v_mul_f32 v90, v90, v226\n"
    "v_mfma_f32_32x32x16_bf16 v[160:175], v[196:199], v[132:135], v[160:175]\n"
    "ds_read_b128 v[196:199], v245 offset:50176\n"
    "v_exp_f32 v183, v183\n"
    "v_mul_f32 v106, v106, v226\n"
    "v_mul_f32 v122, v122, v226\n"
    "s_waitcnt lgkmcnt(2)\n"
    "v_mfma_f32_32x32x16_bf16 v[160:175], v[200:203], v[136:139], v[160:175]\n"
    "ds_read_b128 v[200:203], v244 offset:50688\n"
    "v_exp_f32 v184, v184\n"
    "v_cvt_pk_bf16_f32 v211, v182, v183\n"
    "v_cvt_pk_bf16_f32 v236, v74, v90\n"
    "v_cvt_pk_bf16_f32 v237, v106, v122\n"
    "v_mfma_f32_32x32x16_bf16 v[160:175], v[204:207], v[140:143], v[160:175]\n"
    "ds_read_b128 v[204:207], v245 offset:50688\n"
    "v_exp_f32 v185, v185\n"
    "v_add_f32 v240, v240, v176\n"
    "v_add_f32 v241, v241, v177\n"
    "s_waitcnt lgkmcnt(2)\n"
    "v_mfma_f32_32x32x16_bf16 v[160:175], v[192:195], v[144:147], v[160:175]\n"
    "buffer_store_dwordx2 v[234:235], v250, s[24:27], s45 offen offset:256 sc0 sc1\n"
    "v_exp_f32 v186, v186\n"
    "v_mfma_f32_32x32x16_bf16 v[160:175], v[196:199], v[148:151], v[160:175]\n"
    "v_exp_f32 v187, v187\n"
    "v_cvt_pk_bf16_f32 v212, v184, v185\n"
    "v_add_f32 v240, v240, v178\n"
    "v_add_f32 v241, v241, v179\n"
    "s_waitcnt lgkmcnt(0)\n"
    "v_mfma_f32_32x32x16_bf16 v[160:175], v[200:203], v[152:155], v[160:175]\n"
    "v_exp_f32 v188, v188\n"
    "v_cvt_pk_bf16_f32 v213, v186, v187\n"
    "v_add_f32 v240, v240, v180\n"
    "v_add_f32 v241, v241, v181\n"
    "v_add_f32 v240, v240, v182\n"
    "v_mfma_f32_32x32x16_bf16 v[160:175], v[204:207], v[156:159], v[160:175]\n"
    "buffer_store_dwordx2 v[236:237], v250, s[24:27], s45 offen offset:512 sc0 sc1\n"
    "v_exp_f32 v189, v189\n"
    "v_mfma_f32_32x32x16_bf16 v[0:15], v[208:211], a[160:163], v[0:15]\n"
    "v_exp_f32 v190, v190\n"
    "v_cvt_pk_bf16_f32 v214, v188, v189\n"
    "v_add_f32 v241, v241, v183\n"
    "v_add_f32 v240, v240, v184\n"
    "v_add_f32 v241, v241, v185\n"
    "v_mfma_f32_32x32x16_bf16 v[16:31], v[208:211], a[164:167], v[16:31]\n"
    "v_exp_f32 v191, v191\n"
    "v_add_f32 v240, v240, v186\n"
    "v_add_f32 v241, v241, v187\n"
    "v_add_f32 v240, v240, v188\n"
    "v_add_f32 v241, v241, v189\n"
    "v_mfma_f32_32x32x16_bf16 v[32:47], v[208:211], a[168:171], v[32:47]\n"
    "v_exp_f32 v160, v160\n"
    "v_cvt_pk_bf16_f32 v215, v190, v191\n"
    "v_add_f32 v240, v240, v190\n"
    "v_add_f32 v241, v241, v191\n"
    "v_mul_f32 v75, v75, v227\n"
    "v_mfma_f32_32x32x16_bf16 v[48:63], v[208:211], a[172:175], v[48:63]\n"
    "v_exp_f32 v161, v161\n"
    "v_mul_f32 v91, v91, v227\n"
    "v_mul_f32 v107, v107, v227\n"
    "v_mul_f32 v123, v123, v227\n"
    "v_cvt_pk_bf16_f32 v238, v75, v91\n"
    "v_mfma_f32_32x32x16_bf16 v[0:15], v[212:215], a[176:179], v[0:15]\n"
    "ds_read_b128 v[192:195], v244 offset:57344\n"
    "v_exp_f32 v162, v162\n"
    "v_cvt_pk_bf16_f32 v239, v107, v123\n"
    "v_mul_f32 v76, v76, v228\n"
    "v_mul_f32 v92, v92, v228\n"
    "v_mfma_f32_32x32x16_bf16 v[16:31], v[212:215], a[180:183], v[16:31]\n"
    "ds_read_b128 v[196:199], v245 offset:57344\n"
    "buffer_store_dwordx2 v[238:239], v250, s[24:27], s45 offen offset:768 sc0 sc1\n"
    "v_exp_f32 v163, v163\n"
    "v_mfma_f32_32x32x16_bf16 v[32:47], v[212:215], a[184:187], v[32:47]\n"
    "ds_read_b128 v[200:203], v244 offset:57856\n"
    "v_exp_f32 v164, v164\n"
    "v_mul_f32 v108, v108, v228\n"
    "v_mul_f32 v124, v124, v228\n"
    "v_cvt_pk_bf16_f32 v208, v160, v161\n"
    "v_mfma_f32_32x32x16_bf16 v[48:63], v[212:215], a[188:191], v[48:63]\n"
    "ds_read_b128 v[204:207], v245 offset:57856\n"
    "v_exp_f32 v165, v165\n"
    "v_cvt_pk_bf16_f32 v209, v162, v163\n"
    "v_cvt_pk_bf16_f32 v232, v76, v92\n"
    "s_waitcnt lgkmcnt(2)\n"
    "v_mfma_f32_32x32x16_bf16 v[176:191], v[192:195], v[128:131], 0\n"
    "ds_read_b128 v[192:195], v244 offset:58368\n"
    "s_waitcnt vmcnt(28)\n"
    "v_exp_f32 v166, v166\n"
    "v_cvt_pk_bf16_f32 v210, v164, v165\n"
    "v_cvt_pk_bf16_f32 v233, v108, v124\n"
    "v_mfma_f32_32x32x16_bf16 v[176:191], v[196:199], v[132:135], v[176:191]\n"
    "ds_read_b128 v[196:199], v245 offset:58368\n"
    "v_exp_f32 v167, v167\n"
    "v_add_f32 v240, v240, v160\n"
    "v_add_f32 v241, v241, v161\n"
    "s_waitcnt lgkmcnt(2)\n"
    "v_mfma_f32_32x32x16_bf16 v[176:191], v[200:203], v[136:139], v[176:191]\n"
    "ds_read_b128 v[200:203], v244 offset:58880\n"
    "v_exp_f32 v168, v168\n"
    "v_cvt_pk_bf16_f32 v211, v166, v167\n"
    "v_add_f32 v240, v240, v162\n"
    "v_add_f32 v241, v241, v163\n"
    "v_mfma_f32_32x32x16_bf16 v[176:191], v[204:207], v[140:143], v[176:191]\n"
    "ds_read_b128 v[204:207], v245 offset:58880\n"
    "v_exp_f32 v169, v169\n"
    "v_add_f32 v240, v240, v164\n"
    "v_add_f32 v241, v241, v165\n"
    "s_waitcnt lgkmcnt(2)\n"
    "v_mfma_f32_32x32x16_bf16 v[176:191], v[192:195], v[144:147], v[176:191]\n"
    "buffer_store_dwordx2 v[232:233], v250, s[24:27], s45 offen offset:2048 sc0 sc1\n"
    "v_exp_f32 v170, v170\n"
    "v_mfma_f32_32x32x16_bf16 v[176:191], v[196:199], v[148:151], v[176:191]\n"
    "v_exp_f32 v171, v171\n"
    "v_cvt_pk_bf16_f32 v212, v168, v169\n"
    "v_add_f32 v240, v240, v166\n"
    "v_add_f32 v241, v241, v167\n"
    "s_waitcnt lgkmcnt(0)\n"
    "v_mfma_f32_32x32x16_bf16 v[176:191], v[200:203], v[152:155], v[176:191]\n"
    "ds_read_b128 v[128:131], v246 offset:32768\n"
    "v_exp_f32 v172, v172\n"
    "v_cvt_pk_bf16_f32 v213, v170, v171\n"
    "v_add_f32 v240, v240, v168\n"
    "v_add_f32 v241, v241, v169\n"
    "v_mfma_f32_32x32x16_bf16 v[176:191], v[204:207], v[156:159], v[176:191]\n"
    "ds_read_b128 v[132:135], v247 offset:32768\n"
    "ds_read_b128 v[136:139], v246 offset:33280\n"
    "ds_read_b128 v[140:143], v247 offset:33280\n"
    "v_exp_f32 v173, v173\n"
    "v_add_f32 v240, v240, v170\n"
    "v_mfma_f32_32x32x16_bf16 v[0:15], v[208:211], a[192:195], v[0:15]\n"
    "ds_read_b128 v[144:147], v246 offset:33792\n"
    "ds_read_b128 v[148:151], v247 offset:33792\n"
    "v_exp_f32 v174, v174\n"
    "v_cvt_pk_bf16_f32 v214, v172, v173\n"
    "v_add_f32 v241, v241, v171\n"
    "v_mfma_f32_32x32x16_bf16 v[16:31], v[208:211], a[196:199], v[16:31]\n"
    "ds_read_b128 v[152:155], v246 offset:34304\n"
    "ds_read_b128 v[156:159], v247 offset:34304\n"
    "v_exp_f32 v175, v175\n"
    "v_add_f32 v240, v240, v172\n"
    "v_add_f32 v241, v241, v173\n"
    "v_mfma_f32_32x32x16_bf16 v[32:47], v[208:211], a[200:203], v[32:47]\n"
    "v_exp_f32 v176, v176\n"
    "v_cvt_pk_bf16_f32 v215, v174, v175\n"
    "v_add_f32 v240, v240, v174\n"
    "v_add_f32 v241, v241, v175\n"
    "v_mul_f32 v77, v77, v229\n"
    "v_mfma_f32_32x32x16_bf16 v[48:63], v[208:211], a[204:207], v[48:63]\n"
    "v_exp_f32 v177, v177\n"
    "v_mul_f32 v93, v93, v229\n"
    "v_mul_f32 v109, v109, v229\n"
    "v_mul_f32 v125, v125, v229\n"
    "v_cvt_pk_bf16_f32 v234, v77, v93\n"
    "v_mfma_f32_32x32x16_bf16 v[0:15], v[212:215], a[208:211], v[0:15]\n"
    "ds_read_b128 v[192:195], v244 offset:0\n"
    "v_exp_f32 v178, v178\n"
    "v_cvt_pk_bf16_f32 v235, v109, v125\n"
    "v_mul_f32 v78, v78, v230\n"
    "v_mul_f32 v94, v94, v230\n"
    "v_mfma_f32_32x32x16_bf16 v[16:31], v[212:215], a[212:215], v[16:31]\n"
    "ds_read_b128 v[196:199], v245 offset:0\n"
    "buffer_store_dwordx2 v[234:235], v250, s[24:27], s45 offen offset:2304 sc0 sc1\n"
    "v_exp_f32 v179, v179\n"
    "v_mfma_f32_32x32x16_bf16 v[32:47], v[212:215], a[216:219], v[32:47]\n"
    "ds_read_b128 v[200:203], v244 offset:512\n"
    "v_exp_f32 v180, v180\n"
    "v_mul_f32 v110, v110, v230\n"
    "v_mul_f32 v126, v126, v230\n"
    "v_cvt_pk_bf16_f32 v236, v78, v94\n"
    "v_mfma_f32_32x32x16_bf16 v[48:63], v[212:215], a[220:223], v[48:63]\n"
    "ds_read_b128 v[204:207], v245 offset:512\n"
    "v_exp_f32 v181, v181\n"
    "v_cvt_pk_bf16_f32 v237, v110, v126\n"
    "v_cvt_pk_bf16_f32 v208, v176, v177\n"
    "s_waitcnt lgkmcnt(2)\n"
    "v_mfma_f32_32x32x16_bf16 v[160:175], v[192:195], v[128:131], 0\n"
    "ds_read_b128 v[192:195], v244 offset:1024\n"
    "v_exp_f32 v182, v182\n"
    "v_cvt_pk_bf16_f32 v209, v178, v179\n"
    "v_cvt_pk_bf16_f32 v210, v180, v181\n"
    "v_mul_f32 v79, v79, v231\n"
    "v_mfma_f32_32x32x16_bf16 v[160:175], v[196:199], v[132:135], v[160:175]\n"
    "ds_read_b128 v[196:199], v245 offset:1024\n"
    "v_exp_f32 v183, v183\n"
    "v_mul_f32 v95, v95, v231\n"
    "v_mul_f32 v111, v111, v231\n"
    "s_waitcnt lgkmcnt(2)\n"
    "v_mfma_f32_32x32x16_bf16 v[160:175], v[200:203], v[136:139], v[160:175]\n"
    "ds_read_b128 v[200:203], v244 offset:1536\n"
    "v_exp_f32 v184, v184\n"
    "v_cvt_pk_bf16_f32 v211, v182, v183\n"
    "v_mul_f32 v127, v127, v231\n"
    "v_cvt_pk_bf16_f32 v238, v79, v95\n"
    "v_mfma_f32_32x32x16_bf16 v[160:175], v[204:207], v[140:143], v[160:175]\n"
    "ds_read_b128 v[204:207], v245 offset:1536\n"
    "v_exp_f32 v185, v185\n"
    "v_cvt_pk_bf16_f32 v239, v111, v127\n"
    "s_add_u32 s41, s41, 32768\n"
    "s_waitcnt lgkmcnt(2)\n"
    "v_mfma_f32_32x32x16_bf16 v[160:175], v[192:195], v[144:147], v[160:175]\n"
    "buffer_store_dwordx2 v[236:237], v250, s[24:27], s45 offen offset:2560 sc0 sc1\n"
    "v_exp_f32 v186, v186\n"
    "v_mfma_f32_32x32x16_bf16 v[160:175], v[196:199], v[148:151], v[160:175]\n"
    "v_exp_f32 v187, v187\n"
    "v_cvt_pk_bf16_f32 v212, v184, v185\n"
    "s_add_u32 s42, s42, 32768\n"
    "v_add_f32 v240, v240, v176\n"
    "s_waitcnt lgkmcnt(0)\n"
    "v_mfma_f32_32x32x16_bf16 v[160:175], v[200:203], v[152:155], v[160:175]\n"
    "v_exp_f32 v188, v188\n"
    "v_cvt_pk_bf16_f32 v213, v186, v187\n"
    "v_add_f32 v241, v241, v177\n"
    "v_add_f32 v240, v240, v178\n"
    "v_add_f32 v241, v241, v179\n"
    "v_mfma_f32_32x32x16_bf16 v[160:175], v[204:207], v[156:159], v[160:175]\n"
    "buffer_store_dwordx2 v[238:239], v250, s[24:27], s45 offen offset:2816 sc0 sc1\n"
    "v_exp_f32 v189, v189\n"
    "v_mfma_f32_32x32x16_bf16 v[0:15], v[208:211], a[224:227], v[0:15]\n"
    "v_exp_f32 v190, v190\n"
    "v_cvt_pk_bf16_f32 v214, v188, v189\n"
    "v_add_f32 v240, v240, v180\n"
    "v_add_f32 v241, v241, v181\n"
    "v_add_f32 v240, v240, v182\n"
    "v_mfma_f32_32x32x16_bf16 v[16:31], v[208:211], a[228:231], v[16:31]\n"
    "v_exp_f32 v191, v191\n"
    "v_add_f32 v241, v241, v183\n"
    "v_add_f32 v240, v240, v184\n"
    "v_add_f32 v241, v241, v185\n"
    "v_add_f32 v240, v240, v186\n"
    "v_mfma_f32_32x32x16_bf16 v[32:47], v[208:211], a[232:235], v[32:47]\n"
    "v_exp_f32 v160, v160\n"
    "v_cvt_pk_bf16_f32 v215, v190, v191\n"
    "v_add_f32 v241, v241, v187\n"
    "v_add_f32 v240, v240, v188\n"
    "v_add_f32 v241, v241, v189\n"
    "v_mfma_f32_32x32x16_bf16 v[48:63], v[208:211], a[236:239], v[48:63]\n"
    "v_exp_f32 v161, v161\n"
    "v_add_f32 v240, v240, v190\n"
    "v_add_f32 v241, v241, v191\n"
    "s_add_u32 s52, s52, 1\n"
    "s_branch Lloop_%=\n"
    "Ldrain_%=:\n"
    "v_mfma_f32_32x32x16_bf16 v[64:79], v[212:215], a[208:211], v[64:79]\n"
    "v_exp_f32 v178, v178\n"
    "v_exp_f32 v179, v179\n"
    "v_cvt_pk_bf16_f32 v208, v176, v177\n"
    "v_cvt_pk_bf16_f32 v209, v178, v179\n"
    "v_cvt_pk_bf16_f32 v235, v45, v61\n"
    "v_mul_f32 v14, v14, v230\n"
    "v_mul_f32 v30, v30, v230\n"
    "v_add_f32 v242, v242, v176\n"
    "v_mfma_f32_32x32x16_bf16 v[80:95], v[212:215], a[212:215], v[80:95]\n"
    "buffer_store_dwordx2 v[234:235], v250, s[24:27], s45 offen offset:2304 sc0 sc1\n"
    "v_exp_f32 v180, v180\n"
    "v_exp_f32 v181, v181\n"
    "s_nop 0\n"
    "v_cvt_pk_bf16_f32 v210, v180, v181\n"
    "v_mul_f32 v46, v46, v230\n"
    "v_mfma_f32_32x32x16_bf16 v[96:111], v[212:215], a[216:219], v[96:111]\n"
    "v_exp_f32 v182, v182\n"
    "v_exp_f32 v183, v183\n"
    "s_nop 0\n"
    "v_cvt_pk_bf16_f32 v211, v182, v183\n"
    "v_mul_f32 v62, v62, v230\n"
    "v_cvt_pk_bf16_f32 v236, v14, v30\n"
    "v_cvt_pk_bf16_f32 v237, v46, v62\n"
    "v_mul_f32 v15, v15, v231\n"
    "v_mul_f32 v31, v31, v231\n"
    "v_mfma_f32_32x32x16_bf16 v[112:127], v[212:215], a[220:223], v[112:127]\n"
    "v_exp_f32 v184, v184\n"
    "v_exp_f32 v185, v185\n"
    "v_mul_f32 v47, v47, v231\n"
    "v_mul_f32 v63, v63, v231\n"
    "v_cvt_pk_bf16_f32 v238, v15, v31\n"
    "v_cvt_pk_bf16_f32 v239, v47, v63\n"
    "v_add_f32 v243, v243, v177\n"
    "v_add_f32 v242, v242, v178\n"
    "v_mfma_f32_32x32x16_bf16 v[64:79], v[208:211], a[224:227], v[64:79]\n"
    "buffer_store_dwordx2 v[236:237], v250, s[24:27], s45 offen offset:2560 sc0 sc1\n"
    "v_exp_f32 v186, v186\n"
    "v_exp_f32 v187, v187\n"
    "v_cvt_pk_bf16_f32 v212, v184, v185\n"
    "v_cvt_pk_bf16_f32 v213, v186, v187\n"
    "v_mfma_f32_32x32x16_bf16 v[80:95], v[208:211], a[228:231], v[80:95]\n"
    "buffer_store_dwordx2 v[238:239], v250, s[24:27], s45 offen offset:2816 sc0 sc1\n"
    "v_exp_f32 v188, v188\n"
    "v_exp_f32 v189, v189\n"
    "s_nop 0\n"
    "v_cvt_pk_bf16_f32 v214, v188, v189\n"
    "v_add_f32 v243, v243, v179\n"
    "v_mfma_f32_32x32x16_bf16 v[96:111], v[208:211], a[232:235], v[96:111]\n"
    "v_exp_f32 v190, v190\n"
    "v_exp_f32 v191, v191\n"
    "s_nop 0\n"
    "v_cvt_pk_bf16_f32 v215, v190, v191\n"
    "v_add_f32 v242, v242, v180\n"
    "v_add_f32 v243, v243, v181\n"
    "v_add_f32 v242, v242, v182\n"
    "v_add_f32 v243, v243, v183\n"
    "v_add_f32 v242, v242, v184\n"
    "v_mfma_f32_32x32x16_bf16 v[112:127], v[208:211], a[236:239], v[112:127]\n"
    "v_add_f32 v243, v243, v185\n"
    "v_add_f32 v242, v242, v186\n"
    "v_add_f32 v243, v243, v187\n"
    "v_add_f32 v242, v242, v188\n"
    "v_add_f32 v243, v243, v189\n"
    "v_add_f32 v242, v242, v190\n"
    "v_add_f32 v243, v243, v191\n"
    "v_add_f32 v242, v242, v243\n"
    "v_mov_b32 v255, v242\n"
    "v_mfma_f32_32x32x16_bf16 v[64:79], v[212:215], a[240:243], v[64:79]\n"
    "s_nop 0\n"
    "v_permlane32_swap_b32 v242, v255\n"
    "v_add_f32 v242, v242, v255\n"
    "v_rcp_f32 v255, v242\n"
    "s_add_u32 s45, s45, 32768\n"
    "v_mfma_f32_32x32x16_bf16 v[80:95], v[212:215], a[244:247], v[80:95]\n"
    "ds_write_b32 v251, v255\n"
    "ds_read_b128 v[216:219], v252 offset:0\n"
    "ds_read_b128 v[220:223], v252 offset:32\n"
    "ds_read_b128 v[224:227], v252 offset:64\n"
    "ds_read_b128 v[228:231], v252 offset:96\n"
    "v_max3_f32 v253, v253, v242, v255\n"
    "v_mfma_f32_32x32x16_bf16 v[96:111], v[212:215], a[248:251], v[96:111]\n"
    "v_mfma_f32_32x32x16_bf16 v[112:127], v[212:215], a[252:255], v[112:127]\n"
    "s_waitcnt lgkmcnt(3)\n"
    "v_pk_mul_f32 v[64:65], v[64:65], v[216:217]\n"
    "s_nop 1\n"
    "v_pk_mul_f32 v[80:81], v[80:81], v[216:217]\n"
    "s_nop 5\n"
    "v_pk_mul_f32 v[96:97], v[96:97], v[216:217]\n"
    "v_pk_mul_f32 v[112:113], v[112:113], v[216:217]\n"
    "v_cvt_pk_bf16_f32 v232, v64, v80\n"
    "v_cvt_pk_bf16_f32 v233, v96, v112\n"
    "buffer_store_dwordx2 v[232:233], v250, s[48:51], s45 offen offset:0 sc0 sc1\n"
    "v_cvt_pk_bf16_f32 v234, v65, v81\n"
    "v_cvt_pk_bf16_f32 v235, v97, v113\n"
    "buffer_store_dwordx2 v[234:235], v250, s[48:51], s45 offen offset:256 sc0 sc1\n"
    "v_pk_mul_f32 v[66:67], v[66:67], v[218:219]\n"
    "v_pk_mul_f32 v[82:83], v[82:83], v[218:219]\n"
    "v_pk_mul_f32 v[98:99], v[98:99], v[218:219]\n"
    "v_pk_mul_f32 v[114:115], v[114:115], v[218:219]\n"
    "v_cvt_pk_bf16_f32 v236, v66, v82\n"
    "v_cvt_pk_bf16_f32 v237, v98, v114\n"
    "buffer_store_dwordx2 v[236:237], v250, s[48:51], s45 offen offset:512 sc0 sc1\n"
    "v_cvt_pk_bf16_f32 v238, v67, v83\n"
    "v_cvt_pk_bf16_f32 v239, v99, v115\n"
    "buffer_store_dwordx2 v[238:239], v250, s[48:51], s45 offen offset:768 sc0 sc1\n"
    "s_waitcnt lgkmcnt(2)\n"
    "v_pk_mul_f32 v[68:69], v[68:69], v[220:221]\n"
    "v_pk_mul_f32 v[84:85], v[84:85], v[220:221]\n"
    "v_pk_mul_f32 v[100:101], v[100:101], v[220:221]\n"
    "v_pk_mul_f32 v[116:117], v[116:117], v[220:221]\n"
    "v_cvt_pk_bf16_f32 v232, v68, v84\n"
    "v_cvt_pk_bf16_f32 v233, v100, v116\n"
    "buffer_store_dwordx2 v[232:233], v250, s[48:51], s45 offen offset:2048 sc0 sc1\n"
    "v_cvt_pk_bf16_f32 v234, v69, v85\n"
    "v_cvt_pk_bf16_f32 v235, v101, v117\n"
    "buffer_store_dwordx2 v[234:235], v250, s[48:51], s45 offen offset:2304 sc0 sc1\n"
    "v_pk_mul_f32 v[70:71], v[70:71], v[222:223]\n"
    "v_pk_mul_f32 v[86:87], v[86:87], v[222:223]\n"
    "v_pk_mul_f32 v[102:103], v[102:103], v[222:223]\n"
    "v_pk_mul_f32 v[118:119], v[118:119], v[222:223]\n"
    "v_cvt_pk_bf16_f32 v236, v70, v86\n"
    "v_cvt_pk_bf16_f32 v237, v102, v118\n"
    "buffer_store_dwordx2 v[236:237], v250, s[48:51], s45 offen offset:2560 sc0 sc1\n"
    "v_cvt_pk_bf16_f32 v238, v71, v87\n"
    "v_cvt_pk_bf16_f32 v239, v103, v119\n"
    "buffer_store_dwordx2 v[238:239], v250, s[48:51], s45 offen offset:2816 sc0 sc1\n"
    "s_waitcnt lgkmcnt(1)\n"
    "v_pk_mul_f32 v[72:73], v[72:73], v[224:225]\n"
    "v_pk_mul_f32 v[88:89], v[88:89], v[224:225]\n"
    "v_pk_mul_f32 v[104:105], v[104:105], v[224:225]\n"
    "v_pk_mul_f32 v[120:121], v[120:121], v[224:225]\n"
    "v_cvt_pk_bf16_f32 v232, v72, v88\n"
    "v_cvt_pk_bf16_f32 v233, v104, v120\n"
    "buffer_store_dwordx2 v[232:233], v250, s[24:27], s45 offen offset:0 sc0 sc1\n"
    "v_cvt_pk_bf16_f32 v234, v73, v89\n"
    "v_cvt_pk_bf16_f32 v235, v105, v121\n"
    "buffer_store_dwordx2 v[234:235], v250, s[24:27], s45 offen offset:256 sc0 sc1\n"
    "v_pk_mul_f32 v[74:75], v[74:75], v[226:227]\n"
    "v_pk_mul_f32 v[90:91], v[90:91], v[226:227]\n"
    "v_pk_mul_f32 v[106:107], v[106:107], v[226:227]\n"
    "v_pk_mul_f32 v[122:123], v[122:123], v[226:227]\n"
    "v_cvt_pk_bf16_f32 v236, v74, v90\n"
    "v_cvt_pk_bf16_f32 v237, v106, v122\n"
    "buffer_store_dwordx2 v[236:237], v250, s[24:27], s45 offen offset:512 sc0 sc1\n"
    "v_cvt_pk_bf16_f32 v238, v75, v91\n"
    "v_cvt_pk_bf16_f32 v239, v107, v123\n"
    "buffer_store_dwordx2 v[238:239], v250, s[24:27], s45 offen offset:768 sc0 sc1\n"
    "s_waitcnt lgkmcnt(0)\n"
    "v_pk_mul_f32 v[76:77], v[76:77], v[228:229]\n"
    "v_pk_mul_f32 v[92:93], v[92:93], v[228:229]\n"
    "v_pk_mul_f32 v[108:109], v[108:109], v[228:229]\n"
    "v_pk_mul_f32 v[124:125], v[124:125], v[228:229]\n"
    "v_cvt_pk_bf16_f32 v232, v76, v92\n"
    "v_cvt_pk_bf16_f32 v233, v108, v124\n"
    "buffer_store_dwordx2 v[232:233], v250, s[24:27], s45 offen offset:2048 sc0 sc1\n"
    "v_cvt_pk_bf16_f32 v234, v77, v93\n"
    "v_cvt_pk_bf16_f32 v235, v109, v125\n"
    "buffer_store_dwordx2 v[234:235], v250, s[24:27], s45 offen offset:2304 sc0 sc1\n"
    "v_pk_mul_f32 v[78:79], v[78:79], v[230:231]\n"
    "v_pk_mul_f32 v[94:95], v[94:95], v[230:231]\n"
    "v_pk_mul_f32 v[110:111], v[110:111], v[230:231]\n"
    "v_pk_mul_f32 v[126:127], v[126:127], v[230:231]\n"
    "v_cvt_pk_bf16_f32 v236, v78, v94\n"
    "v_cvt_pk_bf16_f32 v237, v110, v126\n"
    "buffer_store_dwordx2 v[236:237], v250, s[24:27], s45 offen offset:2560 sc0 sc1\n"
    "v_cvt_pk_bf16_f32 v238, v79, v95\n"
    "v_cvt_pk_bf16_f32 v239, v111, v127\n"
    "buffer_store_dwordx2 v[238:239], v250, s[24:27], s45 offen offset:2816 sc0 sc1\n"
    "s_mov_b32 s58, 0x5d800000\n"
    "v_cmp_lt_f32 vcc, s58, v253\n"
    "s_nop 1\n"
    "s_cmp_eq_u64 vcc, 0\n"
    "s_cbranch_scc1 Ldone_%=\n"
    "s_waitcnt vmcnt(0)\n"
    "s_mov_b32 s22, 0x40000\n"
    "v_mbcnt_lo_u32_b32 v0, -1, 0\n"
    "v_mbcnt_hi_u32_b32 v0, -1, v0\n"
    "v_lshrrev_b32 v2, 5, v0\n"
    "v_and_b32 v3, 31, v0\n"
    "v_lshlrev_b32 v248, 8, v3\n"
    "v_lshl_add_u32 v248, v2, 4, v248\n"
    "s_mov_b32 s45, s37\n"
    "s_mov_b32 s52, 0\n"
    "Lslow_%=:\n"
    "buffer_load_dwordx4 v[128:131], v248, s[20:23], s45 offen offset:0\n"
    "buffer_load_dwordx4 v[132:135], v248, s[20:23], s45 offen offset:32\n"
    "buffer_load_dwordx4 v[136:139], v248, s[20:23], s45 offen offset:64\n"
    "buffer_load_dwordx4 v[140:143], v248, s[20:23], s45 offen offset:96\n"
    "buffer_load_dwordx4 v[144:147], v248, s[20:23], s45 offen offset:128\n"
    "buffer_load_dwordx4 v[148:151], v248, s[20:23], s45 offen offset:160\n"
    "buffer_load_dwordx4 v[152:155], v248, s[20:23], s45 offen offset:192\n"
    "buffer_load_dwordx4 v[156:159], v248, s[20:23], s45 offen offset:224\n"
    "s_waitcnt vmcnt(0)\n"
    "v_mov_b32 v240, 0xff800000\n"
    "s_mov_b32 s58, 0\n"
    "v_mov_b32 v232, v244\n"
    "v_mov_b32 v233, v245\n"
    "Lmax_%=:\n"
    "ds_read_b128 v[192:195], v244 offset:0\n"
    "ds_read_b128 v[196:199], v245 offset:0\n"
    "ds_read_b128 v[200:203], v244 offset:512\n"
    "ds_read_b128 v[204:207], v245 offset:512\n"
    "s_waitcnt lgkmcnt(0)\n"
    "v_mfma_f32_32x32x16_bf16 v[160:175], v[192:195], v[128:131], 0\n"
    "v_mfma_f32_32x32x16_bf16 v[160:175], v[196:199], v[132:135], v[160:175]\n"
    "v_mfma_f32_32x32x16_bf16 v[160:175], v[200:203], v[136:139], v[160:175]\n"
    "v_mfma_f32_32x32x16_bf16 v[160:175], v[204:207], v[140:143], v[160:175]\n"
    "s_nop 15\n"
    "ds_read_b128 v[192:195], v244 offset:1024\n"
    "ds_read_b128 v[196:199], v245 offset:1024\n"
    "ds_read_b128 v[200:203], v244 offset:1536\n"
    "ds_read_b128 v[204:207], v245 offset:1536\n"
    "s_waitcnt lgkmcnt(0)\n"
    "v_mfma_f32_32x32x16_bf16 v[160:175], v[192:195], v[144:147], v[160:175]\n"
    "v_mfma_f32_32x32x16_bf16 v[160:175], v[196:199], v[148:151], v[160:175]\n"
    "v_mfma_f32_32x32x16_bf16 v[160:175], v[200:203], v[152:155], v[160:175]\n"
    "v_mfma_f32_32x32x16_bf16 v[160:175], v[204:207], v[156:159], v[160:175]\n"
    "s_nop 15\n"
    "v_max3_f32 v240, v240, v160, v161\n"
    "v_max3_f32 v240, v240, v162, v163\n"
    "v_max3_f32 v240, v240, v164, v165\n"
    "v_max3_f32 v240, v240, v166, v167\n"
    "v_max3_f32 v240, v240, v168, v169\n"
    "v_max3_f32 v240, v240, v170, v171\n"
    "v_max3_f32 v240, v240, v172, v173\n"
    "v_max3_f32 v240, v240, v174, v175\n"
    "v_add_u32 v244, 0x2000, v244\n"
    "v_add_u32 v245, 0x2000, v245\n"
    "s_add_u32 s58, s58, 1\n"
    "s_cmp_lt_u32 s58, 8\n"
    "s_cbranch_scc1 Lmax_%=\n"
    "v_mov_b32 v244, v232\n"
    "v_mov_b32 v245, v233\n"
    "v_mov_b32 v255, v240\n"
    "s_nop 1\n"
    "v_permlane32_swap_b32 v240, v255\n"
    "v_max_f32 v240, v240, v255\n"
    "v_mov_b32 v242, 0\n"
    "ds_read_b128 v[192:195], v244 offset:0\n"
    "ds_read_b128 v[196:199], v245 offset:0\n"
    "ds_read_b128 v[200:203], v244 offset:512\n"
    "ds_read_b128 v[204:207], v245 offset:512\n"
    "s_waitcnt lgkmcnt(0)\n"
    "v_mfma_f32_32x32x16_bf16 v[160:175], v[192:195], v[128:131], 0\n"
    "v_mfma_f32_32x32x16_bf16 v[160:175], v[196:199], v[132:135], v[160:175]\n"
    "v_mfma_f32_32x32x16_bf16 v[160:175], v[200:203], v[136:139], v[160:175]\n"
    "v_mfma_f32_32x32x16_bf16 v[160:175], v[204:207], v[140:143], v[160:175]\n"
    "s_nop 15\n"
    "ds_read_b128 v[192:195], v244 offset:1024\n"
    "ds_read_b128 v[196:199], v245 offset:1024\n"
    "ds_read_b128 v[200:203], v244 offset:1536\n"
    "ds_read_b128 v[204:207], v245 offset:1536\n"
    "s_waitcnt lgkmcnt(0)\n"
    "v_mfma_f32_32x32x16_bf16 v[160:175], v[192:195], v[144:147], v[160:175]\n"
    "v_mfma_f32_32x32x16_bf16 v[160:175], v[196:199], v[148:151], v[160:175]\n"
    "v_mfma_f32_32x32x16_bf16 v[160:175], v[200:203], v[152:155], v[160:175]\n"
    "v_mfma_f32_32x32x16_bf16 v[160:175], v[204:207], v[156:159], v[160:175]\n"
    "s_nop 15\n"
    "v_sub_f32 v160, v160, v240\n"
    "v_sub_f32 v161, v161, v240\n"
    "v_sub_f32 v162, v162, v240\n"
    "v_sub_f32 v163, v163, v240\n"
    "v_sub_f32 v164, v164, v240\n"
    "v_sub_f32 v165, v165, v240\n"
    "v_sub_f32 v166, v166, v240\n"
    "v_sub_f32 v167, v167, v240\n"
    "v_sub_f32 v168, v168, v240\n"
    "v_sub_f32 v169, v169, v240\n"
    "v_sub_f32 v170, v170, v240\n"
    "v_sub_f32 v171, v171, v240\n"
    "v_sub_f32 v172, v172, v240\n"
    "v_sub_f32 v173, v173, v240\n"
    "v_sub_f32 v174, v174, v240\n"
    "v_sub_f32 v175, v175, v240\n"
    "v_exp_f32 v160, v160\n"
    "v_exp_f32 v161, v161\n"
    "v_exp_f32 v162, v162\n"
    "v_exp_f32 v163, v163\n"
    "v_exp_f32 v164, v164\n"
    "v_exp_f32 v165, v165\n"
    "v_exp_f32 v166, v166\n"
    "v_exp_f32 v167, v167\n"
    "v_exp_f32 v168, v168\n"
    "v_exp_f32 v169, v169\n"
    "v_exp_f32 v170, v170\n"
    "v_exp_f32 v171, v171\n"
    "v_exp_f32 v172, v172\n"
    "v_exp_f32 v173, v173\n"
    "v_exp_f32 v174, v174\n"
    "v_exp_f32 v175, v175\n"
    "s_nop 0\n"
    "v_add_f32 v242, v242, v160\n"
    "v_add_f32 v242, v242, v161\n"
    "v_add_f32 v242, v242, v162\n"
    "v_add_f32 v242, v242, v163\n"
    "v_add_f32 v242, v242, v164\n"
    "v_add_f32 v242, v242, v165\n"
    "v_add_f32 v242, v242, v166\n"
    "v_add_f32 v242, v242, v167\n"
    "v_add_f32 v242, v242, v168\n"
    "v_add_f32 v242, v242, v169\n"
    "v_add_f32 v242, v242, v170\n"
    "v_add_f32 v242, v242, v171\n"
    "v_add_f32 v242, v242, v172\n"
    "v_add_f32 v242, v242, v173\n"
    "v_add_f32 v242, v242, v174\n"
    "v_add_f32 v242, v242, v175\n"
    "v_cvt_pk_bf16_f32 v208, v160, v161\n"
    "v_cvt_pk_bf16_f32 v209, v162, v163\n"
    "v_cvt_pk_bf16_f32 v210, v164, v165\n"
    "v_cvt_pk_bf16_f32 v211, v166, v167\n"
    "v_cvt_pk_bf16_f32 v212, v168, v169\n"
    "v_cvt_pk_bf16_f32 v213, v170, v171\n"
    "v_cvt_pk_bf16_f32 v214, v172, v173\n"
    "v_cvt_pk_bf16_f32 v215, v174, v175\n"
    "s_nop 1\n"
    "v_mfma_f32_32x32x16_bf16 v[0:15], v[208:211], a[0:3], 0\n"
    "v_mfma_f32_32x32x16_bf16 v[16:31], v[208:211], a[4:7], 0\n"
    "v_mfma_f32_32x32x16_bf16 v[32:47], v[208:211], a[8:11], 0\n"
    "v_mfma_f32_32x32x16_bf16 v[48:63], v[208:211], a[12:15], 0\n"
    "v_mfma_f32_32x32x16_bf16 v[0:15], v[212:215], a[16:19], v[0:15]\n"
    "v_mfma_f32_32x32x16_bf16 v[16:31], v[212:215], a[20:23], v[16:31]\n"
    "v_mfma_f32_32x32x16_bf16 v[32:47], v[212:215], a[24:27], v[32:47]\n"
    "v_mfma_f32_32x32x16_bf16 v[48:63], v[212:215], a[28:31], v[48:63]\n"
    "s_nop 15\n"
    "ds_read_b128 v[192:195], v244 offset:8192\n"
    "ds_read_b128 v[196:199], v245 offset:8192\n"
    "ds_read_b128 v[200:203], v244 offset:8704\n"
    "ds_read_b128 v[204:207], v245 offset:8704\n"
    "s_waitcnt lgkmcnt(0)\n"
    "v_mfma_f32_32x32x16_bf16 v[160:175], v[192:195], v[128:131], 0\n"
    "v_mfma_f32_32x32x16_bf16 v[160:175], v[196:199], v[132:135], v[160:175]\n"
    "v_mfma_f32_32x32x16_bf16 v[160:175], v[200:203], v[136:139], v[160:175]\n"
    "v_mfma_f32_32x32x16_bf16 v[160:175], v[204:207], v[140:143], v[160:175]\n"
    "s_nop 15\n"
    "ds_read_b128 v[192:195], v244 offset:9216\n"
    "ds_read_b128 v[196:199], v245 offset:9216\n"
    "ds_read_b128 v[200:203], v244 offset:9728\n"
    "ds_read_b128 v[204:207], v245 offset:9728\n"
    "s_waitcnt lgkmcnt(0)\n"
    "v_mfma_f32_32x32x16_bf16 v[160:175], v[192:195], v[144:147], v[160:175]\n"
    "v_mfma_f32_32x32x16_bf16 v[160:175], v[196:199], v[148:151], v[160:175]\n"
    "v_mfma_f32_32x32x16_bf16 v[160:175], v[200:203], v[152:155], v[160:175]\n"
    "v_mfma_f32_32x32x16_bf16 v[160:175], v[204:207], v[156:159], v[160:175]\n"
    "s_nop 15\n"
    "v_sub_f32 v160, v160, v240\n"
    "v_sub_f32 v161, v161, v240\n"
    "v_sub_f32 v162, v162, v240\n"
    "v_sub_f32 v163, v163, v240\n"
    "v_sub_f32 v164, v164, v240\n"
    "v_sub_f32 v165, v165, v240\n"
    "v_sub_f32 v166, v166, v240\n"
    "v_sub_f32 v167, v167, v240\n"
    "v_sub_f32 v168, v168, v240\n"
    "v_sub_f32 v169, v169, v240\n"
    "v_sub_f32 v170, v170, v240\n"
    "v_sub_f32 v171, v171, v240\n"
    "v_sub_f32 v172, v172, v240\n"
    "v_sub_f32 v173, v173, v240\n"
    "v_sub_f32 v174, v174, v240\n"
    "v_sub_f32 v175, v175, v240\n"
    "v_exp_f32 v160, v160\n"
    "v_exp_f32 v161, v161\n"
    "v_exp_f32 v162, v162\n"
    "v_exp_f32 v163, v163\n"
    "v_exp_f32 v164, v164\n"
    "v_exp_f32 v165, v165\n"
    "v_exp_f32 v166, v166\n"
    "v_exp_f32 v167, v167\n"
    "v_exp_f32 v168, v168\n"
    "v_exp_f32 v169, v169\n"
    "v_exp_f32 v170, v170\n"
    "v_exp_f32 v171, v171\n"
    "v_exp_f32 v172, v172\n"
    "v_exp_f32 v173, v173\n"
    "v_exp_f32 v174, v174\n"
    "v_exp_f32 v175, v175\n"
    "s_nop 0\n"
    "v_add_f32 v242, v242, v160\n"
    "v_add_f32 v242, v242, v161\n"
    "v_add_f32 v242, v242, v162\n"
    "v_add_f32 v242, v242, v163\n"
    "v_add_f32 v242, v242, v164\n"
    "v_add_f32 v242, v242, v165\n"
    "v_add_f32 v242, v242, v166\n"
    "v_add_f32 v242, v242, v167\n"
    "v_add_f32 v242, v242, v168\n"
    "v_add_f32 v242, v242, v169\n"
    "v_add_f32 v242, v242, v170\n"
    "v_add_f32 v242, v242, v171\n"
    "v_add_f32 v242, v242, v172\n"
    "v_add_f32 v242, v242, v173\n"
    "v_add_f32 v242, v242, v174\n"
    "v_add_f32 v242, v242, v175\n"
    "v_cvt_pk_bf16_f32 v208, v160, v161\n"
    "v_cvt_pk_bf16_f32 v209, v162, v163\n"
    "v_cvt_pk_bf16_f32 v210, v164, v165\n"
    "v_cvt_pk_bf16_f32 v211, v166, v167\n"
    "v_cvt_pk_bf16_f32 v212, v168, v169\n"
    "v_cvt_pk_bf16_f32 v213, v170, v171\n"
    "v_cvt_pk_bf16_f32 v214, v172, v173\n"
    "v_cvt_pk_bf16_f32 v215, v174, v175\n"
    "s_nop 1\n"
    "v_mfma_f32_32x32x16_bf16 v[0:15], v[208:211], a[32:35], v[0:15]\n"
    "v_mfma_f32_32x32x16_bf16 v[16:31], v[208:211], a[36:39], v[16:31]\n"
    "v_mfma_f32_32x32x16_bf16 v[32:47], v[208:211], a[40:43], v[32:47]\n"
    "v_mfma_f32_32x32x16_bf16 v[48:63], v[208:211], a[44:47], v[48:63]\n"
    "v_mfma_f32_32x32x16_bf16 v[0:15], v[212:215], a[48:51], v[0:15]\n"
    "v_mfma_f32_32x32x16_bf16 v[16:31], v[212:215], a[52:55], v[16:31]\n"
    "v_mfma_f32_32x32x16_bf16 v[32:47], v[212:215], a[56:59], v[32:47]\n"
    "v_mfma_f32_32x32x16_bf16 v[48:63], v[212:215], a[60:63], v[48:63]\n"
    "s_nop 15\n"
    "ds_read_b128 v[192:195], v244 offset:16384\n"
    "ds_read_b128 v[196:199], v245 offset:16384\n"
    "ds_read_b128 v[200:203], v244 offset:16896\n"
    "ds_read_b128 v[204:207], v245 offset:16896\n"
    "s_waitcnt lgkmcnt(0)\n"
    "v_mfma_f32_32x32x16_bf16 v[160:175], v[192:195], v[128:131], 0\n"
    "v_mfma_f32_32x32x16_bf16 v[160:175], v[196:199], v[132:135], v[160:175]\n"
    "v_mfma_f32_32x32x16_bf16 v[160:175], v[200:203], v[136:139], v[160:175]\n"
    "v_mfma_f32_32x32x16_bf16 v[160:175], v[204:207], v[140:143], v[160:175]\n"
    "s_nop 15\n"
    "ds_read_b128 v[192:195], v244 offset:17408\n"
    "ds_read_b128 v[196:199], v245 offset:17408\n"
    "ds_read_b128 v[200:203], v244 offset:17920\n"
    "ds_read_b128 v[204:207], v245 offset:17920\n"
    "s_waitcnt lgkmcnt(0)\n"
    "v_mfma_f32_32x32x16_bf16 v[160:175], v[192:195], v[144:147], v[160:175]\n"
    "v_mfma_f32_32x32x16_bf16 v[160:175], v[196:199], v[148:151], v[160:175]\n"
    "v_mfma_f32_32x32x16_bf16 v[160:175], v[200:203], v[152:155], v[160:175]\n"
    "v_mfma_f32_32x32x16_bf16 v[160:175], v[204:207], v[156:159], v[160:175]\n"
    "s_nop 15\n"
    "v_sub_f32 v160, v160, v240\n"
    "v_sub_f32 v161, v161, v240\n"
    "v_sub_f32 v162, v162, v240\n"
    "v_sub_f32 v163, v163, v240\n"
    "v_sub_f32 v164, v164, v240\n"
    "v_sub_f32 v165, v165, v240\n"
    "v_sub_f32 v166, v166, v240\n"
    "v_sub_f32 v167, v167, v240\n"
    "v_sub_f32 v168, v168, v240\n"
    "v_sub_f32 v169, v169, v240\n"
    "v_sub_f32 v170, v170, v240\n"
    "v_sub_f32 v171, v171, v240\n"
    "v_sub_f32 v172, v172, v240\n"
    "v_sub_f32 v173, v173, v240\n"
    "v_sub_f32 v174, v174, v240\n"
    "v_sub_f32 v175, v175, v240\n"
    "v_exp_f32 v160, v160\n"
    "v_exp_f32 v161, v161\n"
    "v_exp_f32 v162, v162\n"
    "v_exp_f32 v163, v163\n"
    "v_exp_f32 v164, v164\n"
    "v_exp_f32 v165, v165\n"
    "v_exp_f32 v166, v166\n"
    "v_exp_f32 v167, v167\n"
    "v_exp_f32 v168, v168\n"
    "v_exp_f32 v169, v169\n"
    "v_exp_f32 v170, v170\n"
    "v_exp_f32 v171, v171\n"
    "v_exp_f32 v172, v172\n"
    "v_exp_f32 v173, v173\n"
    "v_exp_f32 v174, v174\n"
    "v_exp_f32 v175, v175\n"
    "s_nop 0\n"
    "v_add_f32 v242, v242, v160\n"
    "v_add_f32 v242, v242, v161\n"
    "v_add_f32 v242, v242, v162\n"
    "v_add_f32 v242, v242, v163\n"
    "v_add_f32 v242, v242, v164\n"
    "v_add_f32 v242, v242, v165\n"
    "v_add_f32 v242, v242, v166\n"
    "v_add_f32 v242, v242, v167\n"
    "v_add_f32 v242, v242, v168\n"
    "v_add_f32 v242, v242, v169\n"
    "v_add_f32 v242, v242, v170\n"
    "v_add_f32 v242, v242, v171\n"
    "v_add_f32 v242, v242, v172\n"
    "v_add_f32 v242, v242, v173\n"
    "v_add_f32 v242, v242, v174\n"
    "v_add_f32 v242, v242, v175\n"
    "v_cvt_pk_bf16_f32 v208, v160, v161\n"
    "v_cvt_pk_bf16_f32 v209, v162, v163\n"
    "v_cvt_pk_bf16_f32 v210, v164, v165\n"
    "v_cvt_pk_bf16_f32 v211, v166, v167\n"
    "v_cvt_pk_bf16_f32 v212, v168, v169\n"
    "v_cvt_pk_bf16_f32 v213, v170, v171\n"
    "v_cvt_pk_bf16_f32 v214, v172, v173\n"
    "v_cvt_pk_bf16_f32 v215, v174, v175\n"
    "s_nop 1\n"
    "v_mfma_f32_32x32x16_bf16 v[0:15], v[208:211], a[64:67], v[0:15]\n"
    "v_mfma_f32_32x32x16_bf16 v[16:31], v[208:211], a[68:71], v[16:31]\n"
    "v_mfma_f32_32x32x16_bf16 v[32:47], v[208:211], a[72:75], v[32:47]\n"
    "v_mfma_f32_32x32x16_bf16 v[48:63], v[208:211], a[76:79], v[48:63]\n"
    "v_mfma_f32_32x32x16_bf16 v[0:15], v[212:215], a[80:83], v[0:15]\n"
    "v_mfma_f32_32x32x16_bf16 v[16:31], v[212:215], a[84:87], v[16:31]\n"
    "v_mfma_f32_32x32x16_bf16 v[32:47], v[212:215], a[88:91], v[32:47]\n"
    "v_mfma_f32_32x32x16_bf16 v[48:63], v[212:215], a[92:95], v[48:63]\n"
    "s_nop 15\n"
    "ds_read_b128 v[192:195], v244 offset:24576\n"
    "ds_read_b128 v[196:199], v245 offset:24576\n"
    "ds_read_b128 v[200:203], v244 offset:25088\n"
    "ds_read_b128 v[204:207], v245 offset:25088\n"
    "s_waitcnt lgkmcnt(0)\n"
    "v_mfma_f32_32x32x16_bf16 v[160:175], v[192:195], v[128:131], 0\n"
    "v_mfma_f32_32x32x16_bf16 v[160:175], v[196:199], v[132:135], v[160:175]\n"
    "v_mfma_f32_32x32x16_bf16 v[160:175], v[200:203], v[136:139], v[160:175]\n"
    "v_mfma_f32_32x32x16_bf16 v[160:175], v[204:207], v[140:143], v[160:175]\n"
    "s_nop 15\n"
    "ds_read_b128 v[192:195], v244 offset:25600\n"
    "ds_read_b128 v[196:199], v245 offset:25600\n"
    "ds_read_b128 v[200:203], v244 offset:26112\n"
    "ds_read_b128 v[204:207], v245 offset:26112\n"
    "s_waitcnt lgkmcnt(0)\n"
    "v_mfma_f32_32x32x16_bf16 v[160:175], v[192:195], v[144:147], v[160:175]\n"
    "v_mfma_f32_32x32x16_bf16 v[160:175], v[196:199], v[148:151], v[160:175]\n"
    "v_mfma_f32_32x32x16_bf16 v[160:175], v[200:203], v[152:155], v[160:175]\n"
    "v_mfma_f32_32x32x16_bf16 v[160:175], v[204:207], v[156:159], v[160:175]\n"
    "s_nop 15\n"
    "v_sub_f32 v160, v160, v240\n"
    "v_sub_f32 v161, v161, v240\n"
    "v_sub_f32 v162, v162, v240\n"
    "v_sub_f32 v163, v163, v240\n"
    "v_sub_f32 v164, v164, v240\n"
    "v_sub_f32 v165, v165, v240\n"
    "v_sub_f32 v166, v166, v240\n"
    "v_sub_f32 v167, v167, v240\n"
    "v_sub_f32 v168, v168, v240\n"
    "v_sub_f32 v169, v169, v240\n"
    "v_sub_f32 v170, v170, v240\n"
    "v_sub_f32 v171, v171, v240\n"
    "v_sub_f32 v172, v172, v240\n"
    "v_sub_f32 v173, v173, v240\n"
    "v_sub_f32 v174, v174, v240\n"
    "v_sub_f32 v175, v175, v240\n"
    "v_exp_f32 v160, v160\n"
    "v_exp_f32 v161, v161\n"
    "v_exp_f32 v162, v162\n"
    "v_exp_f32 v163, v163\n"
    "v_exp_f32 v164, v164\n"
    "v_exp_f32 v165, v165\n"
    "v_exp_f32 v166, v166\n"
    "v_exp_f32 v167, v167\n"
    "v_exp_f32 v168, v168\n"
    "v_exp_f32 v169, v169\n"
    "v_exp_f32 v170, v170\n"
    "v_exp_f32 v171, v171\n"
    "v_exp_f32 v172, v172\n"
    "v_exp_f32 v173, v173\n"
    "v_exp_f32 v174, v174\n"
    "v_exp_f32 v175, v175\n"
    "s_nop 0\n"
    "v_add_f32 v242, v242, v160\n"
    "v_add_f32 v242, v242, v161\n"
    "v_add_f32 v242, v242, v162\n"
    "v_add_f32 v242, v242, v163\n"
    "v_add_f32 v242, v242, v164\n"
    "v_add_f32 v242, v242, v165\n"
    "v_add_f32 v242, v242, v166\n"
    "v_add_f32 v242, v242, v167\n"
    "v_add_f32 v242, v242, v168\n"
    "v_add_f32 v242, v242, v169\n"
    "v_add_f32 v242, v242, v170\n"
    "v_add_f32 v242, v242, v171\n"
    "v_add_f32 v242, v242, v172\n"
    "v_add_f32 v242, v242, v173\n"
    "v_add_f32 v242, v242, v174\n"
    "v_add_f32 v242, v242, v175\n"
    "v_cvt_pk_bf16_f32 v208, v160, v161\n"
    "v_cvt_pk_bf16_f32 v209, v162, v163\n"
    "v_cvt_pk_bf16_f32 v210, v164, v165\n"
    "v_cvt_pk_bf16_f32 v211, v166, v167\n"
    "v_cvt_pk_bf16_f32 v212, v168, v169\n"
    "v_cvt_pk_bf16_f32 v213, v170, v171\n"
    "v_cvt_pk_bf16_f32 v214, v172, v173\n"
    "v_cvt_pk_bf16_f32 v215, v174, v175\n"
    "s_nop 1\n"
    "v_mfma_f32_32x32x16_bf16 v[0:15], v[208:211], a[96:99], v[0:15]\n"
    "v_mfma_f32_32x32x16_bf16 v[16:31], v[208:211], a[100:103], v[16:31]\n"
    "v_mfma_f32_32x32x16_bf16 v[32:47], v[208:211], a[104:107], v[32:47]\n"
    "v_mfma_f32_32x32x16_bf16 v[48:63], v[208:211], a[108:111], v[48:63]\n"
    "v_mfma_f32_32x32x16_bf16 v[0:15], v[212:215], a[112:115], v[0:15]\n"
    "v_mfma_f32_32x32x16_bf16 v[16:31], v[212:215], a[116:119], v[16:31]\n"
    "v_mfma_f32_32x32x16_bf16 v[32:47], v[212:215], a[120:123], v[32:47]\n"
    "v_mfma_f32_32x32x16_bf16 v[48:63], v[212:215], a[124:127], v[48:63]\n"
    "s_nop 15\n"
    "ds_read_b128 v[192:195], v244 offset:32768\n"
    "ds_read_b128 v[196:199], v245 offset:32768\n"
    "ds_read_b128 v[200:203], v244 offset:33280\n"
    "ds_read_b128 v[204:207], v245 offset:33280\n"
    "s_waitcnt lgkmcnt(0)\n"
    "v_mfma_f32_32x32x16_bf16 v[160:175], v[192:195], v[128:131], 0\n"
    "v_mfma_f32_32x32x16_bf16 v[160:175], v[196:199], v[132:135], v[160:175]\n"
    "v_mfma_f32_32x32x16_bf16 v[160:175], v[200:203], v[136:139], v[160:175]\n"
    "v_mfma_f32_32x32x16_bf16 v[160:175], v[204:207], v[140:143], v[160:175]\n"
    "s_nop 15\n"
    "ds_read_b128 v[192:195], v244 offset:33792\n"
    "ds_read_b128 v[196:199], v245 offset:33792\n"
    "ds_read_b128 v[200:203], v244 offset:34304\n"
    "ds_read_b128 v[204:207], v245 offset:34304\n"
    "s_waitcnt lgkmcnt(0)\n"
    "v_mfma_f32_32x32x16_bf16 v[160:175], v[192:195], v[144:147], v[160:175]\n"
    "v_mfma_f32_32x32x16_bf16 v[160:175], v[196:199], v[148:151], v[160:175]\n"
    "v_mfma_f32_32x32x16_bf16 v[160:175], v[200:203], v[152:155], v[160:175]\n"
    "v_mfma_f32_32x32x16_bf16 v[160:175], v[204:207], v[156:159], v[160:175]\n"
    "s_nop 15\n"
    "v_sub_f32 v160, v160, v240\n"
    "v_sub_f32 v161, v161, v240\n"
    "v_sub_f32 v162, v162, v240\n"
    "v_sub_f32 v163, v163, v240\n"
    "v_sub_f32 v164, v164, v240\n"
    "v_sub_f32 v165, v165, v240\n"
    "v_sub_f32 v166, v166, v240\n"
    "v_sub_f32 v167, v167, v240\n"
    "v_sub_f32 v168, v168, v240\n"
    "v_sub_f32 v169, v169, v240\n"
    "v_sub_f32 v170, v170, v240\n"
    "v_sub_f32 v171, v171, v240\n"
    "v_sub_f32 v172, v172, v240\n"
    "v_sub_f32 v173, v173, v240\n"
    "v_sub_f32 v174, v174, v240\n"
    "v_sub_f32 v175, v175, v240\n"
    "v_exp_f32 v160, v160\n"
    "v_exp_f32 v161, v161\n"
    "v_exp_f32 v162, v162\n"
    "v_exp_f32 v163, v163\n"
    "v_exp_f32 v164, v164\n"
    "v_exp_f32 v165, v165\n"
    "v_exp_f32 v166, v166\n"
    "v_exp_f32 v167, v167\n"
    "v_exp_f32 v168, v168\n"
    "v_exp_f32 v169, v169\n"
    "v_exp_f32 v170, v170\n"
    "v_exp_f32 v171, v171\n"
    "v_exp_f32 v172, v172\n"
    "v_exp_f32 v173, v173\n"
    "v_exp_f32 v174, v174\n"
    "v_exp_f32 v175, v175\n"
    "s_nop 0\n"
    "v_add_f32 v242, v242, v160\n"
    "v_add_f32 v242, v242, v161\n"
    "v_add_f32 v242, v242, v162\n"
    "v_add_f32 v242, v242, v163\n"
    "v_add_f32 v242, v242, v164\n"
    "v_add_f32 v242, v242, v165\n"
    "v_add_f32 v242, v242, v166\n"
    "v_add_f32 v242, v242, v167\n"
    "v_add_f32 v242, v242, v168\n"
    "v_add_f32 v242, v242, v169\n"
    "v_add_f32 v242, v242, v170\n"
    "v_add_f32 v242, v242, v171\n"
    "v_add_f32 v242, v242, v172\n"
    "v_add_f32 v242, v242, v173\n"
    "v_add_f32 v242, v242, v174\n"
    "v_add_f32 v242, v242, v175\n"
    "v_cvt_pk_bf16_f32 v208, v160, v161\n"
    "v_cvt_pk_bf16_f32 v209, v162, v163\n"
    "v_cvt_pk_bf16_f32 v210, v164, v165\n"
    "v_cvt_pk_bf16_f32 v211, v166, v167\n"
    "v_cvt_pk_bf16_f32 v212, v168, v169\n"
    "v_cvt_pk_bf16_f32 v213, v170, v171\n"
    "v_cvt_pk_bf16_f32 v214, v172, v173\n"
    "v_cvt_pk_bf16_f32 v215, v174, v175\n"
    "s_nop 1\n"
    "v_mfma_f32_32x32x16_bf16 v[0:15], v[208:211], a[128:131], v[0:15]\n"
    "v_mfma_f32_32x32x16_bf16 v[16:31], v[208:211], a[132:135], v[16:31]\n"
    "v_mfma_f32_32x32x16_bf16 v[32:47], v[208:211], a[136:139], v[32:47]\n"
    "v_mfma_f32_32x32x16_bf16 v[48:63], v[208:211], a[140:143], v[48:63]\n"
    "v_mfma_f32_32x32x16_bf16 v[0:15], v[212:215], a[144:147], v[0:15]\n"
    "v_mfma_f32_32x32x16_bf16 v[16:31], v[212:215], a[148:151], v[16:31]\n"
    "v_mfma_f32_32x32x16_bf16 v[32:47], v[212:215], a[152:155], v[32:47]\n"
    "v_mfma_f32_32x32x16_bf16 v[48:63], v[212:215], a[156:159], v[48:63]\n"
    "s_nop 15\n"
    "ds_read_b128 v[192:195], v244 offset:40960\n"
    "ds_read_b128 v[196:199], v245 offset:40960\n"
    "ds_read_b128 v[200:203], v244 offset:41472\n"
    "ds_read_b128 v[204:207], v245 offset:41472\n"
    "s_waitcnt lgkmcnt(0)\n"
    "v_mfma_f32_32x32x16_bf16 v[160:175], v[192:195], v[128:131], 0\n"
    "v_mfma_f32_32x32x16_bf16 v[160:175], v[196:199], v[132:135], v[160:175]\n"
    "v_mfma_f32_32x32x16_bf16 v[160:175], v[200:203], v[136:139], v[160:175]\n"
    "v_mfma_f32_32x32x16_bf16 v[160:175], v[204:207], v[140:143], v[160:175]\n"
    "s_nop 15\n"
    "ds_read_b128 v[192:195], v244 offset:41984\n"
    "ds_read_b128 v[196:199], v245 offset:41984\n"
    "ds_read_b128 v[200:203], v244 offset:42496\n"
    "ds_read_b128 v[204:207], v245 offset:42496\n"
    "s_waitcnt lgkmcnt(0)\n"
    "v_mfma_f32_32x32x16_bf16 v[160:175], v[192:195], v[144:147], v[160:175]\n"
    "v_mfma_f32_32x32x16_bf16 v[160:175], v[196:199], v[148:151], v[160:175]\n"
    "v_mfma_f32_32x32x16_bf16 v[160:175], v[200:203], v[152:155], v[160:175]\n"
    "v_mfma_f32_32x32x16_bf16 v[160:175], v[204:207], v[156:159], v[160:175]\n"
    "s_nop 15\n"
    "v_sub_f32 v160, v160, v240\n"
    "v_sub_f32 v161, v161, v240\n"
    "v_sub_f32 v162, v162, v240\n"
    "v_sub_f32 v163, v163, v240\n"
    "v_sub_f32 v164, v164, v240\n"
    "v_sub_f32 v165, v165, v240\n"
    "v_sub_f32 v166, v166, v240\n"
    "v_sub_f32 v167, v167, v240\n"
    "v_sub_f32 v168, v168, v240\n"
    "v_sub_f32 v169, v169, v240\n"
    "v_sub_f32 v170, v170, v240\n"
    "v_sub_f32 v171, v171, v240\n"
    "v_sub_f32 v172, v172, v240\n"
    "v_sub_f32 v173, v173, v240\n"
    "v_sub_f32 v174, v174, v240\n"
    "v_sub_f32 v175, v175, v240\n"
    "v_exp_f32 v160, v160\n"
    "v_exp_f32 v161, v161\n"
    "v_exp_f32 v162, v162\n"
    "v_exp_f32 v163, v163\n"
    "v_exp_f32 v164, v164\n"
    "v_exp_f32 v165, v165\n"
    "v_exp_f32 v166, v166\n"
    "v_exp_f32 v167, v167\n"
    "v_exp_f32 v168, v168\n"
    "v_exp_f32 v169, v169\n"
    "v_exp_f32 v170, v170\n"
    "v_exp_f32 v171, v171\n"
    "v_exp_f32 v172, v172\n"
    "v_exp_f32 v173, v173\n"
    "v_exp_f32 v174, v174\n"
    "v_exp_f32 v175, v175\n"
    "s_nop 0\n"
    "v_add_f32 v242, v242, v160\n"
    "v_add_f32 v242, v242, v161\n"
    "v_add_f32 v242, v242, v162\n"
    "v_add_f32 v242, v242, v163\n"
    "v_add_f32 v242, v242, v164\n"
    "v_add_f32 v242, v242, v165\n"
    "v_add_f32 v242, v242, v166\n"
    "v_add_f32 v242, v242, v167\n"
    "v_add_f32 v242, v242, v168\n"
    "v_add_f32 v242, v242, v169\n"
    "v_add_f32 v242, v242, v170\n"
    "v_add_f32 v242, v242, v171\n"
    "v_add_f32 v242, v242, v172\n"
    "v_add_f32 v242, v242, v173\n"
    "v_add_f32 v242, v242, v174\n"
    "v_add_f32 v242, v242, v175\n"
    "v_cvt_pk_bf16_f32 v208, v160, v161\n"
    "v_cvt_pk_bf16_f32 v209, v162, v163\n"
    "v_cvt_pk_bf16_f32 v210, v164, v165\n"
    "v_cvt_pk_bf16_f32 v211, v166, v167\n"
    "v_cvt_pk_bf16_f32 v212, v168, v169\n"
    "v_cvt_pk_bf16_f32 v213, v170, v171\n"
    "v_cvt_pk_bf16_f32 v214, v172, v173\n"
    "v_cvt_pk_bf16_f32 v215, v174, v175\n"
    "s_nop 1\n"
    "v_mfma_f32_32x32x16_bf16 v[0:15], v[208:211], a[160:163], v[0:15]\n"
    "v_mfma_f32_32x32x16_bf16 v[16:31], v[208:211], a[164:167], v[16:31]\n"
    "v_mfma_f32_32x32x16_bf16 v[32:47], v[208:211], a[168:171], v[32:47]\n"
    "v_mfma_f32_32x32x16_bf16 v[48:63], v[208:211], a[172:175], v[48:63]\n"
    "v_mfma_f32_32x32x16_bf16 v[0:15], v[212:215], a[176:179], v[0:15]\n"
    "v_mfma_f32_32x32x16_bf16 v[16:31], v[212:215], a[180:183], v[16:31]\n"
    "v_mfma_f32_32x32x16_bf16 v[32:47], v[212:215], a[184:187], v[32:47]\n"
    "v_mfma_f32_32x32x16_bf16 v[48:63], v[212:215], a[188:191], v[48:63]\n"
    "s_nop 15\n"
    "ds_read_b128 v[192:195], v244 offset:49152\n"
    "ds_read_b128 v[196:199], v245 offset:49152\n"
    "ds_read_b128 v[200:203], v244 offset:49664\n"
    "ds_read_b128 v[204:207], v245 offset:49664\n"
    "s_waitcnt lgkmcnt(0)\n"
    "v_mfma_f32_32x32x16_bf16 v[160:175], v[192:195], v[128:131], 0\n"
    "v_mfma_f32_32x32x16_bf16 v[160:175], v[196:199], v[132:135], v[160:175]\n"
    "v_mfma_f32_32x32x16_bf16 v[160:175], v[200:203], v[136:139], v[160:175]\n"
    "v_mfma_f32_32x32x16_bf16 v[160:175], v[204:207], v[140:143], v[160:175]\n"
    "s_nop 15\n"
    "ds_read_b128 v[192:195], v244 offset:50176\n"
    "ds_read_b128 v[196:199], v245 offset:50176\n"
    "ds_read_b128 v[200:203], v244 offset:50688\n"
    "ds_read_b128 v[204:207], v245 offset:50688\n"
    "s_waitcnt lgkmcnt(0)\n"
    "v_mfma_f32_32x32x16_bf16 v[160:175], v[192:195], v[144:147], v[160:175]\n"
    "v_mfma_f32_32x32x16_bf16 v[160:175], v[196:199], v[148:151], v[160:175]\n"
    "v_mfma_f32_32x32x16_bf16 v[160:175], v[200:203], v[152:155], v[160:175]\n"
    "v_mfma_f32_32x32x16_bf16 v[160:175], v[204:207], v[156:159], v[160:175]\n"
    "s_nop 15\n"
    "v_sub_f32 v160, v160, v240\n"
    "v_sub_f32 v161, v161, v240\n"
    "v_sub_f32 v162, v162, v240\n"
    "v_sub_f32 v163, v163, v240\n"
    "v_sub_f32 v164, v164, v240\n"
    "v_sub_f32 v165, v165, v240\n"
    "v_sub_f32 v166, v166, v240\n"
    "v_sub_f32 v167, v167, v240\n"
    "v_sub_f32 v168, v168, v240\n"
    "v_sub_f32 v169, v169, v240\n"
    "v_sub_f32 v170, v170, v240\n"
    "v_sub_f32 v171, v171, v240\n"
    "v_sub_f32 v172, v172, v240\n"
    "v_sub_f32 v173, v173, v240\n"
    "v_sub_f32 v174, v174, v240\n"
    "v_sub_f32 v175, v175, v240\n"
    "v_exp_f32 v160, v160\n"
    "v_exp_f32 v161, v161\n"
    "v_exp_f32 v162, v162\n"
    "v_exp_f32 v163, v163\n"
    "v_exp_f32 v164, v164\n"
    "v_exp_f32 v165, v165\n"
    "v_exp_f32 v166, v166\n"
    "v_exp_f32 v167, v167\n"
    "v_exp_f32 v168, v168\n"
    "v_exp_f32 v169, v169\n"
    "v_exp_f32 v170, v170\n"
    "v_exp_f32 v171, v171\n"
    "v_exp_f32 v172, v172\n"
    "v_exp_f32 v173, v173\n"
    "v_exp_f32 v174, v174\n"
    "v_exp_f32 v175, v175\n"
    "s_nop 0\n"
    "v_add_f32 v242, v242, v160\n"
    "v_add_f32 v242, v242, v161\n"
    "v_add_f32 v242, v242, v162\n"
    "v_add_f32 v242, v242, v163\n"
    "v_add_f32 v242, v242, v164\n"
    "v_add_f32 v242, v242, v165\n"
    "v_add_f32 v242, v242, v166\n"
    "v_add_f32 v242, v242, v167\n"
    "v_add_f32 v242, v242, v168\n"
    "v_add_f32 v242, v242, v169\n"
    "v_add_f32 v242, v242, v170\n"
    "v_add_f32 v242, v242, v171\n"
    "v_add_f32 v242, v242, v172\n"
    "v_add_f32 v242, v242, v173\n"
    "v_add_f32 v242, v242, v174\n"
    "v_add_f32 v242, v242, v175\n"
    "v_cvt_pk_bf16_f32 v208, v160, v161\n"
    "v_cvt_pk_bf16_f32 v209, v162, v163\n"
    "v_cvt_pk_bf16_f32 v210, v164, v165\n"
    "v_cvt_pk_bf16_f32 v211, v166, v167\n"
    "v_cvt_pk_bf16_f32 v212, v168, v169\n"
    "v_cvt_pk_bf16_f32 v213, v170, v171\n"
    "v_cvt_pk_bf16_f32 v214, v172, v173\n"
    "v_cvt_pk_bf16_f32 v215, v174, v175\n"
    "s_nop 1\n"
    "v_mfma_f32_32x32x16_bf16 v[0:15], v[208:211], a[192:195], v[0:15]\n"
    "v_mfma_f32_32x32x16_bf16 v[16:31], v[208:211], a[196:199], v[16:31]\n"
    "v_mfma_f32_32x32x16_bf16 v[32:47], v[208:211], a[200:203], v[32:47]\n"
    "v_mfma_f32_32x32x16_bf16 v[48:63], v[208:211], a[204:207], v[48:63]\n"
    "v_mfma_f32_32x32x16_bf16 v[0:15], v[212:215], a[208:211], v[0:15]\n"
    "v_mfma_f32_32x32x16_bf16 v[16:31], v[212:215], a[212:215], v[16:31]\n"
    "v_mfma_f32_32x32x16_bf16 v[32:47], v[212:215], a[216:219], v[32:47]\n"
    "v_mfma_f32_32x32x16_bf16 v[48:63], v[212:215], a[220:223], v[48:63]\n"
    "s_nop 15\n"
    "ds_read_b128 v[192:195], v244 offset:57344\n"
    "ds_read_b128 v[196:199], v245 offset:57344\n"
    "ds_read_b128 v[200:203], v244 offset:57856\n"
    "ds_read_b128 v[204:207], v245 offset:57856\n"
    "s_waitcnt lgkmcnt(0)\n"
    "v_mfma_f32_32x32x16_bf16 v[160:175], v[192:195], v[128:131], 0\n"
    "v_mfma_f32_32x32x16_bf16 v[160:175], v[196:199], v[132:135], v[160:175]\n"
    "v_mfma_f32_32x32x16_bf16 v[160:175], v[200:203], v[136:139], v[160:175]\n"
    "v_mfma_f32_32x32x16_bf16 v[160:175], v[204:207], v[140:143], v[160:175]\n"
    "s_nop 15\n"
    "ds_read_b128 v[192:195], v244 offset:58368\n"
    "ds_read_b128 v[196:199], v245 offset:58368\n"
    "ds_read_b128 v[200:203], v244 offset:58880\n"
    "ds_read_b128 v[204:207], v245 offset:58880\n"
    "s_waitcnt lgkmcnt(0)\n"
    "v_mfma_f32_32x32x16_bf16 v[160:175], v[192:195], v[144:147], v[160:175]\n"
    "v_mfma_f32_32x32x16_bf16 v[160:175], v[196:199], v[148:151], v[160:175]\n"
    "v_mfma_f32_32x32x16_bf16 v[160:175], v[200:203], v[152:155], v[160:175]\n"
    "v_mfma_f32_32x32x16_bf16 v[160:175], v[204:207], v[156:159], v[160:175]\n"
    "s_nop 15\n"
    "v_sub_f32 v160, v160, v240\n"
    "v_sub_f32 v161, v161, v240\n"
    "v_sub_f32 v162, v162, v240\n"
    "v_sub_f32 v163, v163, v240\n"
    "v_sub_f32 v164, v164, v240\n"
    "v_sub_f32 v165, v165, v240\n"
    "v_sub_f32 v166, v166, v240\n"
    "v_sub_f32 v167, v167, v240\n"
    "v_sub_f32 v168, v168, v240\n"
    "v_sub_f32 v169, v169, v240\n"
    "v_sub_f32 v170, v170, v240\n"
    "v_sub_f32 v171, v171, v240\n"
    "v_sub_f32 v172, v172, v240\n"
    "v_sub_f32 v173, v173, v240\n"
    "v_sub_f32 v174, v174, v240\n"
    "v_sub_f32 v175, v175, v240\n"
    "v_exp_f32 v160, v160\n"
    "v_exp_f32 v161, v161\n"
    "v_exp_f32 v162, v162\n"
    "v_exp_f32 v163, v163\n"
    "v_exp_f32 v164, v164\n"
    "v_exp_f32 v165, v165\n"
    "v_exp_f32 v166, v166\n"
    "v_exp_f32 v167, v167\n"
    "v_exp_f32 v168, v168\n"
    "v_exp_f32 v169, v169\n"
    "v_exp_f32 v170, v170\n"
    "v_exp_f32 v171, v171\n"
    "v_exp_f32 v172, v172\n"
    "v_exp_f32 v173, v173\n"
    "v_exp_f32 v174, v174\n"
    "v_exp_f32 v175, v175\n"
    "s_nop 0\n"
    "v_add_f32 v242, v242, v160\n"
    "v_add_f32 v242, v242, v161\n"
    "v_add_f32 v242, v242, v162\n"
    "v_add_f32 v242, v242, v163\n"
    "v_add_f32 v242, v242, v164\n"
    "v_add_f32 v242, v242, v165\n"
    "v_add_f32 v242, v242, v166\n"
    "v_add_f32 v242, v242, v167\n"
    "v_add_f32 v242, v242, v168\n"
    "v_add_f32 v242, v242, v169\n"
    "v_add_f32 v242, v242, v170\n"
    "v_add_f32 v242, v242, v171\n"
    "v_add_f32 v242, v242, v172\n"
    "v_add_f32 v242, v242, v173\n"
    "v_add_f32 v242, v242, v174\n"
    "v_add_f32 v242, v242, v175\n"
    "v_cvt_pk_bf16_f32 v208, v160, v161\n"
    "v_cvt_pk_bf16_f32 v209, v162, v163\n"
    "v_cvt_pk_bf16_f32 v210, v164, v165\n"
    "v_cvt_pk_bf16_f32 v211, v166, v167\n"
    "v_cvt_pk_bf16_f32 v212, v168, v169\n"
    "v_cvt_pk_bf16_f32 v213, v170, v171\n"
    "v_cvt_pk_bf16_f32 v214, v172, v173\n"
    "v_cvt_pk_bf16_f32 v215, v174, v175\n"
    "s_nop 1\n"
    "v_mfma_f32_32x32x16_bf16 v[0:15], v[208:211], a[224:227], v[0:15]\n"
    "v_mfma_f32_32x32x16_bf16 v[16:31], v[208:211], a[228:231], v[16:31]\n"
    "v_mfma_f32_32x32x16_bf16 v[32:47], v[208:211], a[232:235], v[32:47]\n"
    "v_mfma_f32_32x32x16_bf16 v[48:63], v[208:211], a[236:239], v[48:63]\n"
    "v_mfma_f32_32x32x16_bf16 v[0:15], v[212:215], a[240:243], v[0:15]\n"
    "v_mfma_f32_32x32x16_bf16 v[16:31], v[212:215], a[244:247], v[16:31]\n"
    "v_mfma_f32_32x32x16_bf16 v[32:47], v[212:215], a[248:251], v[32:47]\n"
    "v_mfma_f32_32x32x16_bf16 v[48:63], v[212:215], a[252:255], v[48:63]\n"
    "s_nop 15\n"
    "v_mov_b32 v255, v242\n"
    "s_nop 1\n"
    "v_permlane32_swap_b32 v242, v255\n"
    "v_add_f32 v242, v242, v255\n"
    "v_rcp_f32 v255, v242\n"
    "s_nop 0\n"
    "ds_write_b32 v251, v255\n"
    "ds_read_b128 v[216:219], v252 offset:0\n"
    "ds_read_b128 v[220:223], v252 offset:32\n"
    "ds_read_b128 v[224:227], v252 offset:64\n"
    "ds_read_b128 v[228:231], v252 offset:96\n"
    "s_waitcnt lgkmcnt(0)\n"
    "v_mul_f32 v0, v0, v216\n"
    "v_mul_f32 v16, v16, v216\n"
    "v_mul_f32 v32, v32, v216\n"
    "v_mul_f32 v48, v48, v216\n"
    "v_cvt_pk_bf16_f32 v232, v0, v16\n"
    "v_cvt_pk_bf16_f32 v233, v32, v48\n"
    "buffer_store_dwordx2 v[232:233], v250, s[48:51], s45 offen offset:0\n"
    "v_mul_f32 v1, v1, v217\n"
    "v_mul_f32 v17, v17, v217\n"
    "v_mul_f32 v33, v33, v217\n"
    "v_mul_f32 v49, v49, v217\n"
    "v_cvt_pk_bf16_f32 v234, v1, v17\n"
    "v_cvt_pk_bf16_f32 v235, v33, v49\n"
    "buffer_store_dwordx2 v[234:235], v250, s[48:51], s45 offen offset:256\n"
    "v_mul_f32 v2, v2, v218\n"
    "v_mul_f32 v18, v18, v218\n"
    "v_mul_f32 v34, v34, v218\n"
    "v_mul_f32 v50, v50, v218\n"
    "v_cvt_pk_bf16_f32 v236, v2, v18\n"
    "v_cvt_pk_bf16_f32 v237, v34, v50\n"
    "buffer_store_dwordx2 v[236:237], v250, s[48:51], s45 offen offset:512\n"
    "v_mul_f32 v3, v3, v219\n"
    "v_mul_f32 v19, v19, v219\n"
    "v_mul_f32 v35, v35, v219\n"
    "v_mul_f32 v51, v51, v219\n"
    "v_cvt_pk_bf16_f32 v238, v3, v19\n"
    "v_cvt_pk_bf16_f32 v239, v35, v51\n"
    "buffer_store_dwordx2 v[238:239], v250, s[48:51], s45 offen offset:768\n"
    "v_mul_f32 v4, v4, v220\n"
    "v_mul_f32 v20, v20, v220\n"
    "v_mul_f32 v36, v36, v220\n"
    "v_mul_f32 v52, v52, v220\n"
    "v_cvt_pk_bf16_f32 v232, v4, v20\n"
    "v_cvt_pk_bf16_f32 v233, v36, v52\n"
    "buffer_store_dwordx2 v[232:233], v250, s[48:51], s45 offen offset:2048\n"
    "v_mul_f32 v5, v5, v221\n"
    "v_mul_f32 v21, v21, v221\n"
    "v_mul_f32 v37, v37, v221\n"
    "v_mul_f32 v53, v53, v221\n"
    "v_cvt_pk_bf16_f32 v234, v5, v21\n"
    "v_cvt_pk_bf16_f32 v235, v37, v53\n"
    "buffer_store_dwordx2 v[234:235], v250, s[48:51], s45 offen offset:2304\n"
    "v_mul_f32 v6, v6, v222\n"
    "v_mul_f32 v22, v22, v222\n"
    "v_mul_f32 v38, v38, v222\n"
    "v_mul_f32 v54, v54, v222\n"
    "v_cvt_pk_bf16_f32 v236, v6, v22\n"
    "v_cvt_pk_bf16_f32 v237, v38, v54\n"
    "buffer_store_dwordx2 v[236:237], v250, s[48:51], s45 offen offset:2560\n"
    "v_mul_f32 v7, v7, v223\n"
    "v_mul_f32 v23, v23, v223\n"
    "v_mul_f32 v39, v39, v223\n"
    "v_mul_f32 v55, v55, v223\n"
    "v_cvt_pk_bf16_f32 v238, v7, v23\n"
    "v_cvt_pk_bf16_f32 v239, v39, v55\n"
    "buffer_store_dwordx2 v[238:239], v250, s[48:51], s45 offen offset:2816\n"
    "v_mul_f32 v8, v8, v224\n"
    "v_mul_f32 v24, v24, v224\n"
    "v_mul_f32 v40, v40, v224\n"
    "v_mul_f32 v56, v56, v224\n"
    "v_cvt_pk_bf16_f32 v232, v8, v24\n"
    "v_cvt_pk_bf16_f32 v233, v40, v56\n"
    "buffer_store_dwordx2 v[232:233], v250, s[24:27], s45 offen offset:0\n"
    "v_mul_f32 v9, v9, v225\n"
    "v_mul_f32 v25, v25, v225\n"
    "v_mul_f32 v41, v41, v225\n"
    "v_mul_f32 v57, v57, v225\n"
    "v_cvt_pk_bf16_f32 v234, v9, v25\n"
    "v_cvt_pk_bf16_f32 v235, v41, v57\n"
    "buffer_store_dwordx2 v[234:235], v250, s[24:27], s45 offen offset:256\n"
    "v_mul_f32 v10, v10, v226\n"
    "v_mul_f32 v26, v26, v226\n"
    "v_mul_f32 v42, v42, v226\n"
    "v_mul_f32 v58, v58, v226\n"
    "v_cvt_pk_bf16_f32 v236, v10, v26\n"
    "v_cvt_pk_bf16_f32 v237, v42, v58\n"
    "buffer_store_dwordx2 v[236:237], v250, s[24:27], s45 offen offset:512\n"
    "v_mul_f32 v11, v11, v227\n"
    "v_mul_f32 v27, v27, v227\n"
    "v_mul_f32 v43, v43, v227\n"
    "v_mul_f32 v59, v59, v227\n"
    "v_cvt_pk_bf16_f32 v238, v11, v27\n"
    "v_cvt_pk_bf16_f32 v239, v43, v59\n"
    "buffer_store_dwordx2 v[238:239], v250, s[24:27], s45 offen offset:768\n"
    "v_mul_f32 v12, v12, v228\n"
    "v_mul_f32 v28, v28, v228\n"
    "v_mul_f32 v44, v44, v228\n"
    "v_mul_f32 v60, v60, v228\n"
    "v_cvt_pk_bf16_f32 v232, v12, v28\n"
    "v_cvt_pk_bf16_f32 v233, v44, v60\n"
    "buffer_store_dwordx2 v[232:233], v250, s[24:27], s45 offen offset:2048\n"
    "v_mul_f32 v13, v13, v229\n"
    "v_mul_f32 v29, v29, v229\n"
    "v_mul_f32 v45, v45, v229\n"
    "v_mul_f32 v61, v61, v229\n"
    "v_cvt_pk_bf16_f32 v234, v13, v29\n"
    "v_cvt_pk_bf16_f32 v235, v45, v61\n"
    "buffer_store_dwordx2 v[234:235], v250, s[24:27], s45 offen offset:2304\n"
    "v_mul_f32 v14, v14, v230\n"
    "v_mul_f32 v30, v30, v230\n"
    "v_mul_f32 v46, v46, v230\n"
    "v_mul_f32 v62, v62, v230\n"
    "v_cvt_pk_bf16_f32 v236, v14, v30\n"
    "v_cvt_pk_bf16_f32 v237, v46, v62\n"
    "buffer_store_dwordx2 v[236:237], v250, s[24:27], s45 offen offset:2560\n"
    "v_mul_f32 v15, v15, v231\n"
    "v_mul_f32 v31, v31, v231\n"
    "v_mul_f32 v47, v47, v231\n"
    "v_mul_f32 v63, v63, v231\n"
    "v_cvt_pk_bf16_f32 v238, v15, v31\n"
    "v_cvt_pk_bf16_f32 v239, v47, v63\n"
    "buffer_store_dwordx2 v[238:239], v250, s[24:27], s45 offen offset:2816\n"
    "s_add_u32 s45, s45, 32768\n"
    "s_add_u32 s52, s52, 1\n"
    "s_cmp_lt_u32 s52, 8\n"
    "s_cbranch_scc1 Lslow_%=\n"
    "Ldone_%=:\n"
    : "+{v0}"(tid)
    : "{s[2:3]}"(ka), "{s16}"(blockIdx.x), "{s7}"(ldsaddr)
    : "memory", "vcc", "scc",
      "s4","s5","s6","s8","s9","s10","s11","s12","s13","s14","s15","s17","s18","s19","s20","s21","s22","s23","s24","s25","s26","s27","s28","s29","s30","s31","s34","s35","s36","s37","s38","s39","s40","s41","s42","s43","s44","s45","s46","s47","s48","s49","s50","s51","s52","s53","s54","s55","s56","s57","s58","s59","s60","s61","s62","s63","s64","s65","s66","s67","s68","s69","s70","s71","s72","s73","s74","s75","s76","s77","s78","s79","s80","s81","s82","s83","s84","s85","s86","s87","s88","s89","s90","s91","s92","s93","s94","s95","s96","s97","s98","s99",
      "v1","v2","v3","v4","v5","v6","v7","v8","v9","v10","v11","v12","v13","v14","v15","v16","v17","v18","v19","v20","v21","v22","v23","v24","v25","v26","v27","v28","v29","v30","v31","v32","v33","v34","v35","v36","v37","v38","v39","v40","v41","v42","v43","v44","v45","v46","v47","v48","v49","v50","v51","v52","v53","v54","v55","v56","v57","v58","v59","v60","v61","v62","v63","v64","v65","v66","v67","v68","v69","v70","v71","v72","v73","v74","v75","v76","v77","v78","v79","v80","v81","v82","v83","v84","v85","v86","v87","v88","v89","v90","v91","v92","v93","v94","v95","v96","v97","v98","v99","v100","v101","v102","v103","v104","v105","v106","v107","v108","v109","v110","v111","v112","v113","v114","v115","v116","v117","v118","v119","v120","v121","v122","v123","v124","v125","v126","v127","v128","v129","v130","v131","v132","v133","v134","v135","v136","v137","v138","v139","v140","v141","v142","v143","v144","v145","v146","v147","v148","v149","v150","v151","v152","v153","v154","v155","v156","v157","v158","v159","v160","v161","v162","v163","v164","v165","v166","v167","v168","v169","v170","v171","v172","v173","v174","v175","v176","v177","v178","v179","v180","v181","v182","v183","v184","v185","v186","v187","v188","v189","v190","v191","v192","v193","v194","v195","v196","v197","v198","v199","v200","v201","v202","v203","v204","v205","v206","v207","v208","v209","v210","v211","v212","v213","v214","v215","v216","v217","v218","v219","v220","v221","v222","v223","v224","v225","v226","v227","v228","v229","v230","v231","v232","v233","v234","v235","v236","v237","v238","v239","v240","v241","v242","v243","v244","v245","v246","v247","v248","v249","v250","v251","v252","v253","v254","v255",
      "a0","a1","a2","a3","a4","a5","a6","a7","a8","a9","a10","a11","a12","a13","a14","a15","a16","a17","a18","a19","a20","a21","a22","a23","a24","a25","a26","a27","a28","a29","a30","a31","a32","a33","a34","a35","a36","a37","a38","a39","a40","a41","a42","a43","a44","a45","a46","a47","a48","a49","a50","a51","a52","a53","a54","a55","a56","a57","a58","a59","a60","a61","a62","a63","a64","a65","a66","a67","a68","a69","a70","a71","a72","a73","a74","a75","a76","a77","a78","a79","a80","a81","a82","a83","a84","a85","a86","a87","a88","a89","a90","a91","a92","a93","a94","a95","a96","a97","a98","a99","a100","a101","a102","a103","a104","a105","a106","a107","a108","a109","a110","a111","a112","a113","a114","a115","a116","a117","a118","a119","a120","a121","a122","a123","a124","a125","a126","a127","a128","a129","a130","a131","a132","a133","a134","a135","a136","a137","a138","a139","a140","a141","a142","a143","a144","a145","a146","a147","a148","a149","a150","a151","a152","a153","a154","a155","a156","a157","a158","a159","a160","a161","a162","a163","a164","a165","a166","a167","a168","a169","a170","a171","a172","a173","a174","a175","a176","a177","a178","a179","a180","a181","a182","a183","a184","a185","a186","a187","a188","a189","a190","a191","a192","a193","a194","a195","a196","a197","a198","a199","a200","a201","a202","a203","a204","a205","a206","a207","a208","a209","a210","a211","a212","a213","a214","a215","a216","a217","a218","a219","a220","a221","a222","a223","a224","a225","a226","a227","a228","a229","a230","a231","a232","a233","a234","a235","a236","a237","a238","a239","a240","a241","a242","a243","a244","a245","a246","a247","a248","a249","a250","a251","a252","a253","a254","a255");
}

extern "C" void kernel_launch(void* const* d_in, const int* in_sizes, int n_in, void* d_out, int out_size, void* d_ws, size_t ws_size, hipStream_t stream) {
  standalone__gqa_softmax__near_ridge_kernel<<<256, 256, 0, stream>>>(d_in[0], d_in[1], d_in[2], d_out);
}
```
